```python
import torch

def _softmax(x, axis=-1):
    return torch.softmax(x, dim=axis)
def _gelu(x):
    return torch.nn.functional.gelu(x, approximate="tanh")
def _silu(x):
    return torch.nn.functional.silu(x)
def _sigmoid(x):
    return torch.sigmoid(x)
def _layernorm(x, axis=-1, eps=1e-5):
    m = x.mean(dim=axis, keepdim=True)
    v = x.var(dim=axis, keepdim=True, unbiased=False)
    return (x - m) / torch.sqrt(v + eps)
def _scatter_add(tgt, idx, src, axis=0):
    return tgt.clone().index_add_(axis, idx, src)
def _segment_sum(src, seg, n):
    out = torch.zeros((n,) + src.shape[1:], dtype=src.dtype)
    return out.index_add_(0, seg, src)
def _pad(x, pw):
    flat = tuple(v for pair in reversed(pw) for v in pair)
    return torch.nn.functional.pad(x, flat)

kernel_name = 'sequential_preLN__ssm_conv_s4__mixer_s_mlp__prompt'

def setup_inputs():

    _w = (lambda a: (lambda t: t.to(torch.bfloat16) if t.is_floating_point() else t)(torch.as_tensor(a)))
    def _A_setup():
        B, L, D, N = 1, 2048, 640, 192
        A_re = -(lambda _x: torch.exp(torch.as_tensor(_x)))((lambda *s: torch.randn(*s, dtype=torch.bfloat16) * 6.4)(D, N))
        A_im = torch.pi * (lambda *s: torch.randn(*s, dtype=torch.bfloat16) * 0.05)(D, N)
        B_re = (lambda *s: torch.randn(*s, dtype=torch.bfloat16) * 0.05)(D, N)
        B_im = (lambda *s: torch.randn(*s, dtype=torch.bfloat16) * 0.05)(D, N)
        C_re = (lambda *s: torch.randn(*s, dtype=torch.bfloat16) * 0.05)(D, N)
        C_im = (lambda *s: torch.randn(*s, dtype=torch.bfloat16) * 0.05)(D, N)
        log_dt = (lambda *s: torch.randn(*s, dtype=torch.bfloat16) * 0.05)(D) * 0.5 - 4.0
        Dskip = (lambda *s: torch.randn(*s, dtype=torch.bfloat16) * 0.05)(D)
        return {
            "u": _w((lambda *s: torch.randn(*s, dtype=torch.bfloat16) * 0.05)(B, L, D)),
            "A_re": _w(A_re),
            "A_im": _w(A_im),
            "B_re": _w(B_re),
            "B_im": _w(B_im),
            "C_re": _w(C_re),
            "C_im": _w(C_im),
            "log_dt": _w(log_dt),
            "Dskip": _w(Dskip),
        }
    _A_in = _A_setup()

    def _B_setup():
        B, S, D, F = 1, 2048, 640, 1536
        return {
            "x":  _w((lambda *s: torch.randn(*s, dtype=torch.bfloat16) * 0.05)(B, S, D)),
            "w1": _w((lambda *s: torch.randn(*s, dtype=torch.bfloat16) * 6.4)(D, D) * (D ** -0.5)),
            "w2": _w((lambda *s: torch.randn(*s, dtype=torch.bfloat16) * 6.4)(D, D) * (D ** -0.5)),
            "w3": _w((lambda *s: torch.randn(*s, dtype=torch.bfloat16) * 6.4)(D, F) * (D ** -0.5)),
            "w4": _w((lambda *s: torch.randn(*s, dtype=torch.bfloat16) * 6.4)(F, D) * (F ** -0.5)),
        }
    _B_in = _B_setup()

    return {
        "A_u": _A_in["u"],
        "A_A_re": _A_in["A_re"],
        "A_A_im": _A_in["A_im"],
        "A_B_re": _A_in["B_re"],
        "A_B_im": _A_in["B_im"],
        "A_C_re": _A_in["C_re"],
        "A_C_im": _A_in["C_im"],
        "A_log_dt": _A_in["log_dt"],
        "A_Dskip": _A_in["Dskip"],
        "B_x": _B_in["x"],
        "B_w1": _B_in["w1"],
        "B_w2": _B_in["w2"],
        "B_w3": _B_in["w3"],
        "B_w4": _B_in["w4"],
    }

def reference(A_u, A_A_re, A_A_im, A_B_re, A_B_im, A_C_re, A_C_im, A_log_dt, A_Dskip, B_x, B_w1, B_w2, B_w3, B_w4):
    def _A_ref(u, A_re, A_im, B_re, B_im, C_re, C_im, log_dt, Dskip):
        Bsz, L, D = u.shape
        dt = torch.exp(log_dt)[:, None]
        A = A_re + 1j * A_im
        Bc = B_re + 1j * B_im
        Cc = C_re + 1j * C_im
        logdA = dt * A
        dB = dt * Bc
        l = (lambda x, dt: x.to(dt))(torch.arange(L), A_re.dtype)
        P = torch.exp(logdA[:, :, None] * l[None, None, :])
        K = torch.einsum("dn,dnl,dn->dl", Cc, P, dB).real
        L2 = 2 * L
        Kp = _pad(K, ((0, 0), (0, L)))
        up = (lambda x, *d: x.permute(*d))(u, 0, 2, 1)
        up = _pad(up, ((0, 0), (0, 0), (0, L)))
        Kf = (lambda x, axes=(-2,-1): torch.fft.rfft2(x.float(), dim=axes))(Kp[:, None, :], axes=(-2, -1))
        Uf = (lambda x, axes=(-2,-1): torch.fft.rfft2(x.float(), dim=axes))(up[:, :, None, :], axes=(-2, -1))
        Yf = Uf * Kf[None, :, :, :]
        y = (lambda x, s, axes=(-2,-1): torch.fft.irfft2(x, s=s, dim=axes).to(torch.bfloat16))(Yf, (1, L2), axes=(-2, -1))[:, :, 0, :L]
        y = (lambda x, *d: x.permute(*d))(y, 0, 2, 1) + u * Dskip[None, None, :]
        return y

    def _B_ref(x, w1, w2, w3, w4):
        B, S, D = x.shape
        h = _gelu(_layernorm(x, axis=-1) @ w1)
        c = D // 4
        g0 = _pad(h[:, :, 0*c:1*c], ((0, 0), (1, 0), (0, 0)))[:, :S, :]
        g1 = _pad(h[:, :, 1*c:2*c], ((0, 0), (0, 1), (0, 0)))[:, 1:, :]
        g2 = _pad(h[:, :, 2*c:3*c], ((0, 0), (2, 0), (0, 0)))[:, :S, :]
        g3 = _pad(h[:, :, 3*c:   ], ((0, 0), (0, 2), (0, 0)))[:, 2:, :]
        hs = torch.concatenate([g0, g1, g2, g3], axis=-1) @ w2
        x1 = x + hs
        h2 = _gelu(_layernorm(x1, axis=-1) @ w3) @ w4
        return x1 + h2

    x = A_u
    h = x + _A_ref(_layernorm(x, axis=-1), A_A_re, A_A_im, A_B_re, A_B_im, A_C_re, A_C_im, A_log_dt, A_Dskip)
    h2 = h + _B_ref(_layernorm(h, axis=-1), B_w1, B_w2, B_w3, B_w4)
    return h2
```

```cpp
#include <hip/hip_runtime.h>
#include <hip/hip_bf16.h>
#include <stdint.h>

typedef unsigned short u16;
__device__ __forceinline__ float bf2f(u16 v){ return __uint_as_float(((uint32_t)v)<<16); }
__device__ __forceinline__ u16 f2bf(float f){ uint32_t u=__float_as_uint(f); u += 0x7fffu + ((u>>16)&1); return (u16)(u>>16); }
__device__ __forceinline__ float rbf(float f){ return bf2f(f2bf(f)); }

constexpr int L=2048, D=640, N=192, F=1536;

__global__ void ln_kernel(const u16* in, u16* out){
  int row=blockIdx.x; int lane=threadIdx.x;
  float v[10]; float s=0;
  for(int i=0;i<10;i++){ v[i]=bf2f(in[row*D+lane+64*i]); s+=v[i]; }
  for(int o=32;o;o>>=1) s+=__shfl_xor(s,o);
  float m=s/D; float q=0;
  for(int i=0;i<10;i++){ float d=v[i]-m; q+=d*d; }
  for(int o=32;o;o>>=1) q+=__shfl_xor(q,o);
  float var=rbf(q/D); float mb=rbf(m);
  float sd=rbf(sqrtf(rbf(var+1e-5f)));
  for(int i=0;i<10;i++){ out[row*D+lane+64*i]=f2bf(rbf(v[i]-mb)/sd); }
}

__global__ void kgen_kernel(const u16* Are,const u16* Aim,const u16* Bre,const u16* Bim,const u16* Cre,const u16* Cim,const u16* logdt,float* K){
  int d=blockIdx.y; int l=blockIdx.x*256+threadIdx.x;
  float dt=rbf(expf(bf2f(logdt[d])));
  float lf=rbf((float)l);
  float acc=0;
  for(int n=0;n<N;n++){
    float a=dt*bf2f(Are[d*N+n]), b=dt*bf2f(Aim[d*N+n]);
    float br=dt*bf2f(Bre[d*N+n]), bi=dt*bf2f(Bim[d*N+n]);
    float cr=bf2f(Cre[d*N+n]), ci=bf2f(Cim[d*N+n]);
    float wr=cr*br-ci*bi, wi=cr*bi+ci*br;
    float e=expf(a*lf); float sn,cs; sincosf(b*lf,&sn,&cs);
    acc+= e*(wr*cs-wi*sn);
  }
  K[d*L+l]=acc;
}

__global__ void conv_kernel(const float* K,const u16* u,const u16* x,const u16* Dskip,u16* h){
  int d=blockIdx.x*64+threadIdx.x; int t=blockIdx.y;
  float acc=0;
  for(int s=0;s<=t;s++) acc+=K[d*L+(t-s)]*bf2f(u[s*D+d]);
  float y=rbf(rbf(acc)+rbf(bf2f(u[t*D+d])*bf2f(Dskip[d])));
  h[t*D+d]=f2bf(bf2f(x[t*D+d])+y);
}

__device__ __forceinline__ float gelu(float x){ float u=0.7978845608028654f*(x+0.044715f*x*x*x); return 0.5f*x*(1.f+tanhf(u)); }

__global__ void gemm_kernel(const u16* A,const u16* W,u16* C,int Kk,int Nn,int mode,const u16* res,const u16* res2){
  int n=blockIdx.x*64+threadIdx.x; int t=blockIdx.y;
  float acc=0;
  if(mode==1){
    for(int k=0;k<Kk;k++){ int g=k/160; int sh=(g==0)?-1:(g==1)?1:(g==2)?-2:2; int tt=t+sh; float a=(tt>=0&&tt<L)?bf2f(A[tt*Kk+k]):0.f; acc+=a*bf2f(W[k*Nn+n]); }
    C[t*Nn+n]=f2bf(bf2f(res[t*Nn+n])+rbf(acc));
  } else {
    for(int k=0;k<Kk;k++) acc+=bf2f(A[t*Kk+k])*bf2f(W[k*Nn+n]);
    if(mode==0) C[t*Nn+n]=f2bf(gelu(rbf(acc)));
    else C[t*Nn+n]=f2bf(bf2f(res2[t*Nn+n])+rbf(bf2f(res[t*Nn+n])+rbf(acc)));
  }
}

extern "C" void kernel_launch(void* const* d_in, const int* in_sizes, int n_in,
                              void* d_out, int out_size, void* d_ws, size_t ws_size,
                              hipStream_t stream) {
  const u16* x=(const u16*)d_in[0];
  char* ws=(char*)d_ws;
  u16* u=(u16*)(ws); u16* h=(u16*)(ws+(4<<20)); u16* xb=(u16*)(ws+(8<<20)); u16* xb2=(u16*)(ws+(12<<20));
  u16* g=(u16*)(ws+(16<<20)); u16* x1=(u16*)(ws+(20<<20)); u16* x1n=(u16*)(ws+(24<<20)); u16* H=(u16*)(ws+(28<<20));
  float* K=(float*)(ws+(40<<20));
  ln_kernel<<<L,64,0,stream>>>(x,u);
  kgen_kernel<<<dim3(L/256,D),256,0,stream>>>((const u16*)d_in[1],(const u16*)d_in[2],(const u16*)d_in[3],(const u16*)d_in[4],(const u16*)d_in[5],(const u16*)d_in[6],(const u16*)d_in[7],K);
  conv_kernel<<<dim3(D/64,L),64,0,stream>>>(K,u,x,(const u16*)d_in[8],h);
  ln_kernel<<<L,64,0,stream>>>(h,xb);
  ln_kernel<<<L,64,0,stream>>>(xb,xb2);
  gemm_kernel<<<dim3(D/64,L),64,0,stream>>>(xb2,(const u16*)d_in[10],g,D,D,0,nullptr,nullptr);
  gemm_kernel<<<dim3(D/64,L),64,0,stream>>>(g,(const u16*)d_in[11],x1,D,D,1,xb,nullptr);
  ln_kernel<<<L,64,0,stream>>>(x1,x1n);
  gemm_kernel<<<dim3(F/64,L),64,0,stream>>>(x1n,(const u16*)d_in[12],H,D,F,0,nullptr,nullptr);
  gemm_kernel<<<dim3(D/64,L),64,0,stream>>>(H,(const u16*)d_in[13],(u16*)d_out,F,D,2,x1,h);
}
```

```cpp
#include <hip/hip_runtime.h>
#include <stdint.h>

typedef unsigned short u16;
typedef unsigned v4u __attribute__((ext_vector_type(4)));
typedef unsigned v2u __attribute__((ext_vector_type(2)));
typedef float f32x4 __attribute__((ext_vector_type(4)));
typedef float f32x2 __attribute__((ext_vector_type(2)));
typedef short s16x8 __attribute__((ext_vector_type(8)));
typedef short s16x4 __attribute__((ext_vector_type(4)));
typedef __bf16 bf16x2 __attribute__((ext_vector_type(2)));
typedef __bf16 bf16x8 __attribute__((ext_vector_type(8)));
#define GAS __attribute__((address_space(1)))
#define LAS __attribute__((address_space(3)))
typedef GAS unsigned gu32;
#define RLX_AGENT __ATOMIC_RELAXED, __HIP_MEMORY_SCOPE_AGENT
#define DEV __device__ __forceinline__

constexpr int L = 2048, D = 640, NS = 192, F = 1536;
constexpr float EPS = 1e-5f;
constexpr int RSTR = 2112;

constexpr int WS_CTRL = 0;
constexpr int WS_CS1 = 32768;
constexpr int WS_CS3 = WS_CS1 + 4096;
constexpr int WS_ST2 = 65536;
constexpr int WS_UT = WS_ST2 + 131072;
constexpr int WS_XT = WS_UT + D * L * 2;
constexpr int WS_RK = WS_XT + D * L * 2;
constexpr int WS_HT = WS_RK + D * RSTR * 2;
constexpr int WS_W1 = WS_HT + D * L * 2;
constexpr int WS_W2 = WS_W1 + D * D * 2;
constexpr int WS_W3 = WS_W2 + D * D * 2;
constexpr int WS_W4 = WS_W3 + D * F * 2;
constexpr int WS_GS = WS_W4 + D * F * 2;
constexpr int WS_X1 = WS_GS + D * L * 2;
constexpr int WS_HF = WS_X1 + D * L * 2;
constexpr int WS_END = WS_HF + F * L * 2;

DEV float bflo(unsigned v) { return __uint_as_float(v << 16); }
DEV float bfhi(unsigned v) { return __uint_as_float(v & 0xffff0000u); }
DEV float bf2f(u16 v) { return __uint_as_float(((unsigned)v) << 16); }
DEV unsigned pk2(float lo, float hi) {
  f32x2 f = {lo, hi};
  return __builtin_bit_cast(unsigned, __builtin_convertvector(f, bf16x2));
}
DEV f32x4 mfma(v4u a, v4u b, f32x4 c) {
  return __builtin_amdgcn_mfma_f32_16x16x32_bf16(__builtin_bit_cast(bf16x8, a), __builtin_bit_cast(bf16x8, b), c, 0, 0, 0);
}
DEV v2u trread(LAS char* p) {
  return __builtin_bit_cast(v2u, __builtin_amdgcn_ds_read_tr16_b64_v4i16((LAS s16x4*)p));
}
DEV float dot2(unsigned a, unsigned b, float c) {
  return __builtin_amdgcn_fdot2_f32_bf16(__builtin_bit_cast(bf16x2, a), __builtin_bit_cast(bf16x2, b), c, false);
}
DEV void wave_sync() {
  __builtin_amdgcn_fence(__ATOMIC_RELEASE, "wavefront");
  __builtin_amdgcn_wave_barrier();
  __builtin_amdgcn_fence(__ATOMIC_ACQUIRE, "wavefront");
}
DEV void wg_barrier() {
  asm volatile("s_waitcnt lgkmcnt(0)" ::: "memory");
  __builtin_amdgcn_s_barrier();
  asm volatile("" ::: "memory");
}
DEV float gelu(float v) {
  float p = __builtin_fmaf(v * v, 0.1029432397f, 2.302208198f);
  float e = __builtin_amdgcn_exp2f(-v * p);
  return v * __builtin_amdgcn_rcpf(1.0f + e);
}

struct Ptrs {
  const u16 *x, *are, *aim, *bre, *bim, *cre, *cim, *logdt, *dskip, *w1, *w2, *w3, *w4;
  u16* out;
  char* ws;
};

constexpr int PREP_LN = 64, PREP_KG = 640, PREP_PK = 256;
constexpr int PREP_LDS = 25600;

DEV void prep_ln0(const Ptrs& P, LAS char* S, int b) {
  const int tid = threadIdx.x, lane = tid & 63, w = tid >> 6;
  const int row = tid >> 3, j = tid & 7, t0 = b * 32;
  const GAS v4u* xp = (const GAS v4u*)(P.x + (size_t)(t0 + row) * D) + j;
  v4u xv[10];
#pragma unroll
  for (int i = 0; i < 10; ++i) xv[i] = xp[8 * i];
  float s = 0.f;
#pragma unroll
  for (int i = 0; i < 10; ++i)
#pragma unroll
    for (int e = 0; e < 4; ++e) s += bflo(xv[i][e]) + bfhi(xv[i][e]);
  s += __shfl_xor(s, 1); s += __shfl_xor(s, 2); s += __shfl_xor(s, 4);
  const float m = s * (1.0f / D);
  float q = 0.f;
#pragma unroll
  for (int i = 0; i < 10; ++i)
#pragma unroll
    for (int e = 0; e < 4; ++e) {
      float a = bflo(xv[i][e]) - m, c = bfhi(xv[i][e]) - m;
      q = __builtin_fmaf(a, a, q); q = __builtin_fmaf(c, c, q);
    }
  q += __shfl_xor(q, 1); q += __shfl_xor(q, 2); q += __shfl_xor(q, 4);
  const float rs = __builtin_amdgcn_rsqf(q * (1.0f / D) + EPS);
  constexpr int STR = 656;
  const int r = lane & 15, g = lane >> 4, qq = r >> 2, pp = lane & 3;
#pragma unroll
  for (int pass = 0; pass < 4; ++pass) {
    const int hf = pass & 1, isu = pass >> 1;
    if (pass) wg_barrier();
#pragma unroll
    for (int i = 0; i < 5; ++i) {
      v4u v = xv[5 * hf + i];
      if (isu) {
#pragma unroll
        for (int e = 0; e < 4; ++e) v[e] = pk2((bflo(v[e]) - m) * rs, (bfhi(v[e]) - m) * rs);
      }
      *(LAS v4u*)(S + row * STR + (j + 8 * i) * 16) = v;
    }
    wg_barrier();
    GAS char* dst = (GAS char*)P.ws + (isu ? WS_UT : WS_XT);
#pragma unroll
    for (int i = 0; i < 5; ++i) {
      const int dt = w * 5 + i;
      LAS char* a = S + (8 * g + qq) * STR + dt * 32 + pp * 8;
      v2u lo = trread(a), hi = trread(a + 4 * STR);
      v4u o = {lo[0], lo[1], hi[0], hi[1]};
      *(GAS v4u*)(dst + ((size_t)(320 * hf + dt * 16 + r) * L + t0 + 8 * g) * 2) = o;
    }
  }
  if (b == 0) {
    GAS unsigned* cw = (GAS unsigned*)(P.ws + WS_CTRL);
    if (tid < 96) cw[tid * 64] = 0u;
    if (tid < 120) {
      const int gq = tid & 3, it = tid >> 2;
      int ks, rw;
      if (it < 5) { ks = it; rw = 0; }
      else if (it < 10) { ks = it; rw = L - 1; }
      else if (it < 20) { ks = 10 + (it - 10) % 5; rw = (it - 10) / 5; }
      else { ks = 15 + (it - 20) % 5; rw = L - 2 + (it - 20) / 5; }
      v4u z = {0u, 0u, 0u, 0u};
      *(GAS v4u*)(P.ws + WS_GS + (((rw >> 4) * 20 + ks) * 64 + (rw & 15) + 16 * gq) * 16) = z;
    }
  }
}

DEV int nmap80(int odd, int nt, int p) {
  return nt < 4 ? (odd ? 16 : 0) + 32 * (nt >> 1) + 8 * p + 4 * (nt & 1) : (odd ? 0 : 64) + 4 * p;
}
DEV int nmap48(int odd, int nt, int p) {
  return nt < 2 ? (odd ? 16 : 0) + 8 * p + 4 * nt : (odd ? 0 : 32) + 4 * p;
}

DEV void prep_pack(const Ptrs& P, LAS char* S, int u) {
  const int tid = threadIdx.x, lane = tid & 63, w = tid >> 6;
  const int row = tid >> 3, j = tid & 7;
  const int g = lane >> 4, qq = (lane & 15) >> 2, pp = lane & 3;
  constexpr int STR = 784;
  if (u < 176) {
    const u16* W; int dstoff, KS, uu;
    if (u < 40) { W = P.w1; dstoff = WS_W1; KS = 20; uu = u; }
    else if (u < 80) { W = P.w2; dstoff = WS_W2; KS = 20; uu = u - 40; }
    else { W = P.w4; dstoff = WS_W4; KS = 48; uu = u - 80; }
    const int ks = uu >> 1, hf = uu & 1;
    const GAS v4u* src = (const GAS v4u*)(W + (size_t)(32 * ks + row) * D + 320 * hf) + j;
    v4u v[5];
#pragma unroll
    for (int i = 0; i < 5; ++i) v[i] = src[8 * i];
#pragma unroll
    for (int i = 0; i < 5; ++i) *(LAS v4u*)(S + row * STR + (j + 8 * i) * 16) = v[i];
    wg_barrier();
    const int c = 4 * hf + w;
#pragma unroll
    for (int nt = 0; nt < 5; ++nt) {
      LAS char* a = S + (8 * g + qq) * STR + (80 * w + nmap80(c & 1, nt, pp)) * 2;
      v2u lo = trread(a), hi = trread(a + 4 * STR);
      v4u o = {lo[0], lo[1], hi[0], hi[1]};
      *(GAS v4u*)(P.ws + dstoff + (((c * KS + ks) * 5 + nt) * 64 + lane) * 16) = o;
    }
  } else {
    const int uu = u - 176, ks = uu >> 2, qt = uu & 3;
    const GAS v4u* src = (const GAS v4u*)(P.w3 + (size_t)(32 * ks + row) * F + 384 * qt) + j;
    v4u v[6];
#pragma unroll
    for (int i = 0; i < 6; ++i) v[i] = src[8 * i];
#pragma unroll
    for (int i = 0; i < 6; ++i) *(LAS v4u*)(S + row * STR + (j + 8 * i) * 16) = v[i];
    wg_barrier();
#pragma unroll
    for (int h = 0; h < 2; ++h) {
      const int wg = 2 * w + h;
      const int cw = 8 * qt + wg;
#pragma unroll
      for (int nt = 0; nt < 3; ++nt) {
        LAS char* a = S + (8 * g + qq) * STR + (48 * wg + nmap48(h, nt, pp)) * 2;
        v2u lo = trread(a), hi = trread(a + 4 * STR);
        v4u o = {lo[0], lo[1], hi[0], hi[1]};
        *(GAS v4u*)(P.ws + WS_W3 + (((cw * 20 + ks) * 3 + nt) * 64 + lane) * 16) = o;
      }
    }
  }
}

struct cf { float re, im; };
DEV cf cmul(cf a, cf b) {
  return {__builtin_fmaf(a.re, b.re, -a.im * b.im), __builtin_fmaf(a.re, b.im, a.im * b.re)};
}
DEV cf cexpl(float a2, float br, float l) {
  float e = __builtin_amdgcn_exp2f(a2 * l);
  float ph = __builtin_amdgcn_fractf(br * l);
  return {e * __builtin_amdgcn_cosf(ph), e * __builtin_amdgcn_sinf(ph)};
}

DEV void prep_kgen(const Ptrs& P, LAS char* S, int d) {
  const int tid = threadIdx.x, lane = tid & 63, w = tid >> 6;
  constexpr int TSTR = 272;
  constexpr int TB = 16 * TSTR;
  LAS float* part = (LAS float*)(S + 3 * TB);
  LAS u16* tab = (LAS u16*)(S + 3 * TB + 3 * 704 * 4);
  if (w < 3) {
    const int i = d * NS + tid;
    const float are = bf2f(P.are[i]), aim = bf2f(P.aim[i]), bre = bf2f(P.bre[i]), bim = bf2f(P.bim[i]);
    const float cre = bf2f(P.cre[i]), cim = bf2f(P.cim[i]);
    const float dt = bflo(pk2(expf(bf2f(P.logdt[d])), 0.f));
    const float a2 = dt * are * 1.4426950408889634f, br = dt * aim * 0.15915494309189535f;
    const cf Wc = cmul(cf{cre, cim}, cf{dt * bre, dt * bim});
    LAS char* T = S + w * TB;
    LAS char* wr = T + lane * 4;
    LAS char* rd = T + (lane & 15) * TSTR + (lane >> 4) * 16;
    const cf e1 = cexpl(a2, br, 1.f), e16 = cexpl(a2, br, 16.f), e256 = cexpl(a2, br, 256.f);
    {
      const cf e32 = cmul(e16, e16);
      cf h0 = Wc, h1 = cmul(Wc, e16);
#pragma unroll
      for (int a = 0; a < 16; a += 2) {
        *(LAS unsigned*)(wr + a * TSTR) = pk2(h0.re, -h0.im);
        *(LAS unsigned*)(wr + (a + 1) * TSTR) = pk2(h1.re, -h1.im);
        if (a < 14) { h0 = cmul(h0, e32); h1 = cmul(h1, e32); }
      }
    }
    wave_sync();
    v4u af[4];
#pragma unroll
    for (int k = 0; k < 4; ++k) af[k] = *(LAS v4u*)(rd + k * 64);
    wave_sync();
    f32x4 acc[3];
    cf ev[8];
    {
      const cf e2 = cmul(e1, e1);
      ev[0] = cf{1.f, 0.f}; ev[1] = e2; ev[2] = cmul(e2, e2); ev[3] = cmul(ev[2], e2);
      ev[4] = cmul(ev[2], ev[2]); ev[5] = cmul(ev[4], e2); ev[6] = cmul(ev[4], ev[2]); ev[7] = cmul(ev[4], ev[3]);
#pragma unroll
      for (int c = 0; c < 8; ++c) {
        const cf o = c ? cmul(ev[c], e1) : e1;
        *(LAS unsigned*)(wr + (2 * c) * TSTR) = pk2(ev[c].re, ev[c].im);
        *(LAS unsigned*)(wr + (2 * c + 1) * TSTR) = pk2(o.re, o.im);
      }
    }
#define KG_MMA(T_)                                                            \
  {                                                                           \
    wave_sync();                                                              \
    v4u bf[4];                                                                \
    _Pragma("unroll") for (int k = 0; k < 4; ++k) bf[k] = *(LAS v4u*)(rd + k * 64); \
    wave_sync();                                                              \
    f32x4 z = {0.f, 0.f, 0.f, 0.f};                                           \
    _Pragma("unroll") for (int k = 0; k < 4; ++k) z = mfma(af[k], bf[k], z);  \
    acc[T_] = z;                                                              \
  }
    KG_MMA(0)
    const cf e512 = cmul(e256, e256), e768 = cmul(e512, e256), e1024 = cmul(e512, e512);
#pragma unroll
    for (int c = 0; c < 8; ++c) {
      const cf o = c ? cmul(e256, ev[c]) : e256;
      *(LAS unsigned*)(wr + c * TSTR) = pk2(o.re, o.im);
    }
#pragma unroll
    for (int c = 0; c < 4; ++c) {
      const cf o = c ? cmul(e512, ev[2 * c]) : e512, p = c ? cmul(e768, ev[2 * c]) : e768;
      *(LAS unsigned*)(wr + (8 + c) * TSTR) = pk2(o.re, o.im);
      *(LAS unsigned*)(wr + (12 + c) * TSTR) = pk2(p.re, p.im);
    }
    KG_MMA(1)
    {
      cf bs[4] = {e1024, cmul(e1024, e256), cmul(e1024, e512), cmul(e1024, e768)};
#pragma unroll
      for (int c = 0; c < 4; ++c) {
        const cf o = cmul(bs[c], ev[4]);
        *(LAS unsigned*)(wr + (2 * c) * TSTR) = pk2(bs[c].re, bs[c].im);
        *(LAS unsigned*)(wr + (2 * c + 1) * TSTR) = pk2(o.re, o.im);
      }
      const cf e2048 = cmul(e1024, e1024);
      *(LAS unsigned*)(wr + 8 * TSTR) = pk2(e2048.re, e2048.im);
    }
    KG_MMA(2)
#undef KG_MMA
    const int r = lane & 15, g = lane >> 4;
    LAS float* pw = part + w * 704;
    {
      int b1, s1, b2, s2;
      if (r < 8) { b1 = 256 + r; s1 = 8; b2 = 512 + 32 * (r >> 1) + (r & 1); s2 = 2; }
      else { b1 = 384 + 64 * ((r - 8) >> 2) + (r & 3); s1 = 4; b2 = (r == 8) ? 640 : 672; s2 = 1; }
#pragma unroll
      for (int i = 0; i < 4; ++i) {
        const int a = 4 * g + i;
        pw[16 * a + r] = acc[0][i];
        pw[b1 + s1 * a] = acc[1][i];
        pw[b2 + s2 * a] = acc[2][i];
      }
    }
  }
  wg_barrier();
#pragma unroll
  for (int jj = 0; jj < 3; ++jj) {
    const int e = tid + 256 * jj;
    if (e <= 640) tab[e] = (u16)(pk2(part[e] + part[704 + e] + part[1408 + e], 0.f) & 0xffffu);
  }
  wg_barrier();
  {
    unsigned o[4];
#pragma unroll
    for (int e = 0; e < 4; ++e) {
      const int l0 = 2047 - 8 * tid - 2 * e, l1 = l0 - 1;
      const unsigned bb = pk2((float)l0, (float)l1);
      const int i0 = l0 < 128 ? l0 : (int)(bb & 0xffffu) - 0x4300 + 128;
      const int i1 = l1 < 128 ? l1 : (int)(bb >> 16) - 0x4300 + 128;
      o[e] = (unsigned)tab[i0] | ((unsigned)tab[i1] << 16);
    }
    GAS v4u* dst = (GAS v4u*)(P.ws + WS_RK + (size_t)d * RSTR * 2);
    dst[tid] = v4u{o[0], o[1], o[2], o[3]};
    if (tid < 8) dst[256 + tid] = v4u{0u, 0u, 0u, 0u};
  }
}

__global__ void __launch_bounds__(256) k_prep(Ptrs P) {
  extern __shared__ __attribute__((aligned(16))) char smem[];
  LAS char* S = (LAS char*)smem;
  const int b = blockIdx.x;
  if (b < PREP_LN) prep_ln0(P, S, b);
  else if (b < PREP_LN + PREP_KG) prep_kgen(P, S, b - PREP_LN);
  else prep_pack(P, S, b - PREP_LN - PREP_KG);
}

constexpr int CV_KSTR = 4160, CV_USTR = 4640, CV_WAVE = 4 * CV_KSTR + 2 * CV_USTR;
constexpr int CV_LDS = 4 * CV_WAVE;
constexpr int CV_BLOCKS = 160, CV_CS = 68;

DEV void colsum_block(const Ptrs& P, LAS char* S, int u) {
  const int tid = threadIdx.x;
  const u16* W; int ld, c0, dst;
  if (u < 20) { W = P.w1; ld = D; c0 = 32 * u; dst = WS_CS1; }
  else { W = P.w3; ld = F; c0 = 32 * (u - 20); dst = WS_CS3; }
  const int j = tid & 3, rg = tid >> 2;
  v4u v[10];
#pragma unroll
  for (int i = 0; i < 10; ++i) v[i] = *(const GAS v4u*)(W + (size_t)(rg + 64 * i) * ld + c0 + 8 * j);
  float s[8];
#pragma unroll
  for (int e = 0; e < 8; ++e) s[e] = 0.f;
#pragma unroll
  for (int i = 0; i < 10; ++i)
#pragma unroll
    for (int e = 0; e < 4; ++e) { s[2 * e] += bflo(v[i][e]); s[2 * e + 1] += bfhi(v[i][e]); }
  LAS float* R = (LAS float*)S;
#pragma unroll
  for (int e = 0; e < 8; ++e) R[rg * 33 + 8 * j + e] = s[e];
  wg_barrier();
  if (tid < 32) {
    float a = 0.f;
    for (int i = 0; i < 64; ++i) a += R[i * 33 + tid];
    *(GAS float*)(P.ws + dst + (c0 + tid) * 4) = a;
  }
}

__global__ void __launch_bounds__(256, 1) k_conv(Ptrs P) {
  extern __shared__ __attribute__((aligned(16))) char smem[];
  LAS char* S = (LAS char*)smem;
  if (blockIdx.x >= CV_BLOCKS) { colsum_block(P, S, blockIdx.x - CV_BLOCKS); return; }
  const int lane = threadIdx.x & 63, w = __builtin_amdgcn_readfirstlane(threadIdx.x >> 6);
  const int d = blockIdx.x * 4 + w;
  LAS char* KB = S + w * CV_WAVE;
  LAS char* UB = KB + 4 * CV_KSTR;
  const int r = lane & 15, g = lane >> 4;
  const GAS char* rk = (const GAS char*)P.ws + WS_RK + (size_t)d * RSTR * 2;
  const GAS char* ut = (const GAS char*)P.ws + WS_UT + (size_t)d * L * 2;
  const GAS char* xt = (const GAS char*)P.ws + WS_XT + (size_t)d * L * 2;
  v4u kv[4], uv[4]; v2u kn[4], xv[8];
#pragma unroll
  for (int i = 0; i < 4; ++i) {
    kv[i] = *(const GAS v4u*)(rk + i * 1024 + lane * 16);
    kn[i] = *(const GAS v2u*)(rk + i * 1024 + lane * 16 + 16);
    uv[i] = *(const GAS v4u*)(ut + i * 1024 + lane * 16);
  }
  const int toff = 8 * g + 4 * (r & 1) + 32 * (r >> 1);
#pragma unroll
  for (int b = 0; b < 8; ++b) xv[b] = *(const GAS v2u*)(xt + (256 * b + toff) * 2);
  const float dsk = bf2f(P.dskip[d]);
  {
    v4u z = {0u, 0u, 0u, 0u};
    if (lane < 16) *(LAS v4u*)(KB + (lane >> 2) * CV_KSTR + 4096 + (lane & 3) * 16) = z;
    *(LAS v4u*)(UB + (lane >> 5) * CV_USTR + (lane & 31) * 16) = z;
    if (lane == 0) *(LAS v2u*)(UB + 512) = v2u{0u, 0u};
  }
#pragma unroll
  for (int i = 0; i < 4; ++i) {
    const unsigned d0 = kv[i][0], d1 = kv[i][1], d2 = kv[i][2], d3 = kv[i][3], d4 = kn[i][0], d5 = kn[i][1];
    const unsigned o0 = __builtin_amdgcn_alignbit(d1, d0, 16), o1 = __builtin_amdgcn_alignbit(d2, d1, 16),
                   o2 = __builtin_amdgcn_alignbit(d3, d2, 16), o3 = __builtin_amdgcn_alignbit(d4, d3, 16),
                   o4 = __builtin_amdgcn_alignbit(d5, d4, 16);
    LAS char* p = KB + i * 1024 + lane * 16;
    *(LAS v4u*)(p + 3 * CV_KSTR) = v4u{d0, d1, d2, d3};
    *(LAS v4u*)(p + 2 * CV_KSTR) = v4u{o0, o1, o2, o3};
    *(LAS v4u*)(p + 1 * CV_KSTR) = v4u{d1, d2, d3, d4};
    *(LAS v4u*)(p + 0 * CV_KSTR) = v4u{o1, o2, o3, o4};
    LAS char* q = UB + 512 + i * 1024 + lane * 16;
    *(LAS v4u*)(q + CV_USTR) = uv[i];
    *(LAS v2u*)(q + 8) = v2u{uv[i][0], uv[i][1]};
    *(LAS v2u*)(q + 16) = v2u{uv[i][2], uv[i][3]};
  }
  wave_sync();
  LAS char* pa = KB + (r & 3) * CV_KSTR + (2040 - 32 * 56 + 8 * g - 8 * (r >> 2)) * 2;
  LAS char* pb = UB + (r & 1) * CV_USTR + (256 + 32 * (r >> 1) + 8 * g) * 2;
  f32x4 acc[8];
#pragma unroll
  for (int b = 0; b < 8; ++b) acc[b] = f32x4{0.f, 0.f, 0.f, 0.f};
  v4u ta[2][8], tb[2][8];
#pragma unroll
  for (int i = 0; i < 8; ++i) {
    ta[0][i] = *(LAS v4u*)(pa + (7 - i) * 512);
    tb[0][i] = *(LAS v4u*)(pb + i * 512);
  }
#pragma unroll 1
  for (int rho = 0; rho < 8; rho += 2) {
#pragma unroll
    for (int h = 0; h < 2; ++h) {
      pa -= 64; pb -= 64;
      if (h == 0 || rho < 6) {
#pragma unroll
        for (int i = 0; i < 8; ++i) {
          ta[h ^ 1][i] = *(LAS v4u*)(pa + (7 - i) * 512);
          tb[h ^ 1][i] = *(LAS v4u*)(pb + i * 512);
        }
      }
#pragma unroll
      for (int ga = 0; ga < 8; ++ga)
#pragma unroll
        for (int de = 0; de + ga < 8; ++de) acc[ga + de] = mfma(ta[h][ga], tb[h][de], acc[ga + de]);
    }
  }
  GAS char* ht = (GAS char*)P.ws + WS_HT + (size_t)d * L * 2;
#pragma unroll
  for (int b = 0; b < 8; ++b) {
    const v2u uu = *(LAS v2u*)(UB + CV_USTR + 512 + (256 * b + toff) * 2);
    const float h0 = bflo(xv[b][0]) + __builtin_fmaf(bflo(uu[0]), dsk, acc[b][0]);
    const float h1 = bfhi(xv[b][0]) + __builtin_fmaf(bfhi(uu[0]), dsk, acc[b][1]);
    const float h2 = bflo(xv[b][1]) + __builtin_fmaf(bflo(uu[1]), dsk, acc[b][2]);
    const float h3 = bfhi(xv[b][1]) + __builtin_fmaf(bfhi(uu[1]), dsk, acc[b][3]);
    *(GAS v2u*)(ht + (256 * b + toff) * 2) = v2u{pk2(h0, h1), pk2(h2, h3)};
  }
}

constexpr int ML_TILE = 0;
constexpr int ML_RED = 81920;
constexpr int ML_STAT = ML_RED + 61440;
constexpr int ML_MR = ML_STAT + 2048;
constexpr int ML_LDS = ML_MR + 512;
constexpr int AUX_SC1 = 16;
#define SB() __builtin_amdgcn_sched_barrier(0)

DEV void publish(gu32* c0, gu32* c1, bool two) {
  asm volatile("s_waitcnt vmcnt(0)" ::: "memory");
  wg_barrier();
  if (threadIdx.x < (two ? 2u : 1u)) __hip_atomic_fetch_add(threadIdx.x ? c1 : c0, 1u, RLX_AGENT);
}
DEV void wait_ge(gu32* c, unsigned want) {
  if (threadIdx.x == 0) {
    unsigned spins = 0;
    while (__hip_atomic_load(c, RLX_AGENT) < want && ++spins < (1u << 22)) __builtin_amdgcn_s_sleep(1);
    __builtin_amdgcn_fence(__ATOMIC_ACQUIRE, "wavefront");
  }
  wg_barrier();
}

DEV void reduce_write(LAS char* S, int w, int lane, f32x4 (&acc)[5][4]) {
  LAS char* p = S + ML_RED + w * 15360 + lane * 16;
#pragma unroll
  for (int tau = 1; tau < 4; ++tau)
#pragma unroll
    for (int nt = 0; nt < 5; ++nt) *(LAS f32x4*)(p + ((tau - 1) * 5 + nt) * 1024) = acc[nt][tau];
}
DEV void reduce_read(LAS char* S, int w, int lane, f32x4 (&acc)[5][4]) {
#pragma unroll
  for (int tau = 1; tau < 4; ++tau) {
    LAS char* p = S + ML_RED + ((w - tau) & 3) * 15360 + (tau - 1) * 5120 + lane * 16;
#pragma unroll
    for (int nt = 0; nt < 5; ++nt) acc[nt][0] += *(LAS f32x4*)(p + nt * 1024);
  }
}

__global__ void __launch_bounds__(256, 1) k_mlp(Ptrs P) {
  extern __shared__ __attribute__((aligned(16))) char smem[];
  LAS char* S = (LAS char*)smem;
  const int lane = threadIdx.x & 63, w = __builtin_amdgcn_readfirstlane(threadIdx.x >> 6);
  const int bx = blockIdx.x, pn = (bx & 7) * 4 + (bx >> 6), c = (bx >> 3) & 7;
  const int r = lane & 15, g = lane >> 4, odd = c & 1;
  const __amdgpu_buffer_rsrc_t rs = __builtin_amdgcn_make_buffer_rsrc((void*)P.ws, 0, WS_END, 0x00020000);
  gu32* ctr = (gu32*)(P.ws + WS_CTRL);
  const int l16 = lane * 16;
  const int pb = odd ? 16 : 0, sb = odd ? 0 : 64;

  v4u hs[5][4], w1f[25], w2f[25];
  {
    const int hoff = WS_HT + ((160 * w + (lane >> 3)) * L + 64 * pn) * 2 + (lane & 7) * 16;
    const int woff = WS_W1 + (c * 20 + 5 * w) * 5120 + l16;
#pragma unroll
    for (int k = 0; k < 5; ++k) {
#pragma unroll
      for (int i = 0; i < 4; ++i) hs[k][i] = __builtin_amdgcn_raw_buffer_load_b128(rs, hoff + (32 * k + 8 * i) * L * 2, 0, 0);
#pragma unroll
      for (int nt = 0; nt < 5; ++nt) w1f[k * 5 + nt] = __builtin_amdgcn_raw_buffer_load_b128(rs, woff + (k * 5 + nt) * 1024, 0, 0);
    }
  }
  {
    const int woff = WS_W2 + (c * 20 + 5 * w) * 5120 + l16;
#pragma unroll
    for (int i = 0; i < 25; ++i) w2f[i] = __builtin_amdgcn_raw_buffer_load_b128(rs, woff + i * 1024, 0, 0);
  }
  SB();
  f32x4 acc[5][4];
#pragma unroll
  for (int nt = 0; nt < 5; ++nt)
#pragma unroll
    for (int t = 0; t < 4; ++t) acc[nt][t] = f32x4{0.f, 0.f, 0.f, 0.f};
  float s1[4] = {0.f, 0.f, 0.f, 0.f}, s2[4] = {0.f, 0.f, 0.f, 0.f};
  {
    LAS char* tw = S + ML_TILE + w * 20480;
    const int rw = lane >> 3;
    LAS char* wp0 = tw + rw * 128 + (((lane & 7) ^ (((rw >> 1) & 1) << 1)) << 4);
    LAS char* wp1 = tw + rw * 128 + (((lane & 7) ^ ((((rw >> 1) & 1) | 2) << 1)) << 4);
    const int q = r >> 2, p = lane & 3, f = (q >> 1) | ((g & 1) << 1);
    LAS char* rp[4];
#pragma unroll
    for (int t = 0; t < 4; ++t) rp[t] = tw + (8 * g + q) * 128 + ((((t + w) & 3) ^ f) << 5) + 8 * p;
#pragma unroll
    for (int k = 0; k < 5; ++k) {
#pragma unroll
      for (int i = 0; i < 4; ++i) *(LAS v4u*)(((i & 1) ? wp1 : wp0) + k * 4096 + i * 1024) = hs[k][i];
      wave_sync();
#pragma unroll
      for (int t = 0; t < 4; ++t) {
        const v2u lo = trread(rp[t] + k * 4096), hi = trread(rp[t] + k * 4096 + 512);
        const v4u bf = {lo[0], lo[1], hi[0], hi[1]};
#pragma unroll
        for (int nt = 0; nt < 5; ++nt) acc[nt][t] = mfma(w1f[k * 5 + nt], bf, acc[nt][t]);
#pragma unroll
        for (int e = 0; e < 4; ++e) { s1[t] = dot2(bf[e], 0x3f803f80u, s1[t]); s2[t] = dot2(bf[e], bf[e], s2[t]); }
      }
    }
  }
  f32x4 cs1[5];
#pragma unroll
  for (int nt = 0; nt < 5; ++nt)
    cs1[nt] = __builtin_bit_cast(f32x4, __builtin_amdgcn_raw_buffer_load_b128(rs, WS_CS1 + (80 * c + nmap80(odd, nt, g)) * 4, 0, 0));
  v4u w3f[60];
  const int w3off = WS_W3 + (c * 4 + w) * 61440 + l16;
#pragma unroll
  for (int i = 0; i < 24; ++i) w3f[i] = __builtin_amdgcn_raw_buffer_load_b128(rs, w3off + i * 1024, 0, 0);
  SB();
  reduce_write(S, w, lane, acc);
#pragma unroll
  for (int t = 0; t < 4; ++t) {
    float a = s1[t], b = s2[t];
    a += __shfl_xor(a, 16); b += __shfl_xor(b, 16);
    a += __shfl_xor(a, 32); b += __shfl_xor(b, 32);
    if (g == 0) *(LAS f32x2*)(S + ML_STAT + (w * 64 + ((t + w) & 3) * 16 + r) * 8) = f32x2{a, b};
  }
  wg_barrier();
  reduce_read(S, w, lane, acc);
  float m1, r1;
  v2u hres[5];
  {
    f32x2 st = {0.f, 0.f};
#pragma unroll
    for (int i = 0; i < 4; ++i) st += *(LAS f32x2*)(S + ML_STAT + (i * 64 + w * 16 + r) * 8);
    m1 = st[0] * (1.0f / D);
    const float var = fmaxf(st[1] * (1.0f / D) - m1 * m1, 0.f);
    r1 = __builtin_amdgcn_rsqf(var + EPS);
    const float r2 = __builtin_amdgcn_rsqf(var * r1 * r1 + EPS);
    const float rr = r1 * r2;
    const int q = r >> 2, p = lane & 3;
#pragma unroll
    for (int nt = 0; nt < 5; ++nt) {
      const int row = 80 * c + nmap80(odd, nt, g) + q;
      const int f = ((row >> 1) & 1) | (((row >> 3) & 1) << 1);
      hres[nt] = trread(S + ML_TILE + row * 128 + ((w ^ f) << 5) + 8 * p);
    }
    unsigned o[10];
#pragma unroll
    for (int nt = 0; nt < 5; ++nt) {
      float v[4];
#pragma unroll
      for (int i = 0; i < 4; ++i) v[i] = gelu(__builtin_fmaf(-m1, cs1[nt][i], acc[nt][0][i]) * rr);
      o[2 * nt] = pk2(v[0], v[1]); o[2 * nt + 1] = pk2(v[2], v[3]);
    }
    const int grp = c >> 1, sh = (grp == 0) ? 1 : (grp == 1) ? -1 : (grp == 2) ? 2 : -2;
    const int td = 64 * pn + 16 * w + r + sh;
    if (td >= 0 && td < L) {
      const int rowoff = WS_GS + (td >> 4) * 20480 + (td & 15) * 16;
      const int kp = (80 * c + pb) >> 5, ksg = (80 * c + sb) >> 5;
      __builtin_amdgcn_raw_buffer_store_b128(v4u{o[0], o[1], o[2], o[3]}, rs, rowoff + kp * 1024 + g * 256, 0, AUX_SC1);
      __builtin_amdgcn_raw_buffer_store_b128(v4u{o[4], o[5], o[6], o[7]}, rs, rowoff + (kp + 1) * 1024 + g * 256, 0, AUX_SC1);
      __builtin_amdgcn_raw_buffer_store_b64(v2u{o[8], o[9]}, rs, rowoff + ksg * 1024 + (2 * odd + (g >> 1)) * 256 + (g & 1) * 8, 0, AUX_SC1);
    }
    const int nb = pn + (sh > 0 ? 1 : -1);
    publish(ctr + pn * 64, ctr + (nb & 31) * 64, nb >= 0 && nb < 32);
  }
  wait_ge(ctr + pn * 64, (pn == 0 || pn == 31) ? 12u : 16u);
  {
    v4u af[5][4];
#pragma unroll
    for (int k = 0; k < 5; ++k)
#pragma unroll
      for (int t = 0; t < 4; ++t)
        af[k][t] = __builtin_amdgcn_raw_buffer_load_b128(rs, WS_GS + ((4 * pn + ((t + w) & 3)) * 20 + 5 * w + k) * 1024 + l16, 0, AUX_SC1);
    SB();
#pragma unroll
    for (int nt = 0; nt < 5; ++nt)
#pragma unroll
      for (int t = 0; t < 4; ++t) acc[nt][t] = f32x4{0.f, 0.f, 0.f, 0.f};
#pragma unroll
    for (int k = 0; k < 5; ++k)
#pragma unroll
      for (int t = 0; t < 4; ++t)
#pragma unroll
        for (int nt = 0; nt < 5; ++nt) acc[nt][t] = mfma(w2f[k * 5 + nt], af[k][t], acc[nt][t]);
  }
#pragma unroll
  for (int i = 24; i < 60; ++i) w3f[i] = __builtin_amdgcn_raw_buffer_load_b128(rs, w3off + i * 1024, 0, 0);
  SB();
  reduce_write(S, w, lane, acc);
  wg_barrier();
  reduce_read(S, w, lane, acc);
  float res[20];
  {
    unsigned o[10];
    float a = 0.f, b = 0.f;
#pragma unroll
    for (int nt = 0; nt < 5; ++nt) {
      const float h0 = bflo(hres[nt][0]), h1 = bfhi(hres[nt][0]), h2 = bflo(hres[nt][1]), h3 = bfhi(hres[nt][1]);
      o[2 * nt] = pk2(__builtin_fmaf(h0 - m1, r1, acc[nt][0][0]), __builtin_fmaf(h1 - m1, r1, acc[nt][0][1]));
      o[2 * nt + 1] = pk2(__builtin_fmaf(h2 - m1, r1, acc[nt][0][2]), __builtin_fmaf(h3 - m1, r1, acc[nt][0][3]));
      res[4 * nt + 0] = h0 + bflo(o[2 * nt]); res[4 * nt + 1] = h1 + bfhi(o[2 * nt]);
      res[4 * nt + 2] = h2 + bflo(o[2 * nt + 1]); res[4 * nt + 3] = h3 + bfhi(o[2 * nt + 1]);
      a = dot2(o[2 * nt], 0x3f803f80u, a); a = dot2(o[2 * nt + 1], 0x3f803f80u, a);
      b = dot2(o[2 * nt], o[2 * nt], b); b = dot2(o[2 * nt + 1], o[2 * nt + 1], b);
    }
    const int rowoff = WS_X1 + (4 * pn + w) * 20480 + r * 16;
    const int kp = (80 * c + pb) >> 5, ksg = (80 * c + sb) >> 5;
    __builtin_amdgcn_raw_buffer_store_b128(v4u{o[0], o[1], o[2], o[3]}, rs, rowoff + kp * 1024 + g * 256, 0, AUX_SC1);
    __builtin_amdgcn_raw_buffer_store_b128(v4u{o[4], o[5], o[6], o[7]}, rs, rowoff + (kp + 1) * 1024 + g * 256, 0, AUX_SC1);
    __builtin_amdgcn_raw_buffer_store_b64(v2u{o[8], o[9]}, rs, rowoff + ksg * 1024 + (2 * odd + (g >> 1)) * 256 + (g & 1) * 8, 0, AUX_SC1);
    a += __shfl_xor(a, 16); b += __shfl_xor(b, 16);
    a += __shfl_xor(a, 32); b += __shfl_xor(b, 32);
    if (g == 0)
      __builtin_amdgcn_raw_buffer_store_b64(v2u{__float_as_uint(a), __float_as_uint(b)}, rs, WS_ST2 + ((64 * pn + 16 * w + r) * 8 + c) * 8, 0, AUX_SC1);
    publish(ctr + (32 + pn) * 64, ctr, false);
  }
  wait_ge(ctr + (32 + pn) * 64, 8u);
  v4u w4f[60];
  const int w4off = WS_W4 + (c * 48 + 12 * w) * 5120 + l16;
  {
    v4u xs[20];
#pragma unroll
    for (int k = 0; k < 20; ++k) xs[k] = __builtin_amdgcn_raw_buffer_load_b128(rs, WS_X1 + ((4 * pn + w) * 20 + k) * 1024 + l16, 0, AUX_SC1);
    const f32x4 stp = __builtin_bit_cast(f32x4, __builtin_amdgcn_raw_buffer_load_b128(rs, WS_ST2 + (64 * pn + 16 * w + r) * 64 + g * 16, 0, AUX_SC1));
    f32x4 cs3[3];
#pragma unroll
    for (int nt = 0; nt < 3; ++nt)
      cs3[nt] = __builtin_bit_cast(f32x4, __builtin_amdgcn_raw_buffer_load_b128(rs, WS_CS3 + (192 * c + 48 * w + nmap48(w & 1, nt, g)) * 4, 0, 0));
    SB();
    f32x4 a3[3][4];
#pragma unroll
    for (int nt = 0; nt < 3; ++nt)
#pragma unroll
      for (int t = 0; t < 4; ++t) a3[nt][t] = f32x4{0.f, 0.f, 0.f, 0.f};
#pragma unroll
    for (int s = 0; s < 4; ++s) {
#pragma unroll
      for (int k = 5 * s; k < 5 * s + 5; ++k) *(LAS v4u*)(S + ML_TILE + (k * 4 + w) * 1024 + l16) = xs[k];
      if (s == 3) {
        float a = stp[0] + stp[2], b = stp[1] + stp[3];
        a += __shfl_xor(a, 16); b += __shfl_xor(b, 16);
        a += __shfl_xor(a, 32); b += __shfl_xor(b, 32);
        const float m = a * (1.0f / D);
        const float var = fmaxf(b * (1.0f / D) - m * m, 0.f);
        if (g == 0) *(LAS f32x2*)(S + ML_MR + (16 * w + r) * 8) = f32x2{m, __builtin_amdgcn_rsqf(var + EPS)};
      }
      wg_barrier();
#pragma unroll
      for (int k = 5 * s; k < 5 * s + 5; ++k) {
#pragma unroll
        for (int t = 0; t < 4; ++t) {
          const v4u bf = *(LAS v4u*)(S + ML_TILE + (k * 4 + t) * 1024 + l16);
#pragma unroll
          for (int nt = 0; nt < 3; ++nt) a3[nt][t] = mfma(w3f[k * 3 + nt], bf, a3[nt][t]);
        }
#pragma unroll
        for (int nt = 0; nt < 3; ++nt) w4f[k * 3 + nt] = __builtin_amdgcn_raw_buffer_load_b128(rs, w4off + (k * 3 + nt) * 1024, 0, 0);
      }
      SB();
    }
    const int wo = w & 1;
    const int n0 = 192 * c + 48 * w;
    const int kp = (n0 + (wo ? 16 : 0)) >> 5, ksg = (n0 + (wo ? 0 : 32)) >> 5;
#pragma unroll
    for (int t = 0; t < 4; ++t) {
      const f32x2 mr = *(LAS f32x2*)(S + ML_MR + (16 * t + r) * 8);
      unsigned o[6];
#pragma unroll
      for (int nt = 0; nt < 3; ++nt) {
        float v[4];
#pragma unroll
        for (int i = 0; i < 4; ++i) v[i] = gelu(__builtin_fmaf(-mr[0], cs3[nt][i], a3[nt][t][i]) * mr[1]);
        o[2 * nt] = pk2(v[0], v[1]); o[2 * nt + 1] = pk2(v[2], v[3]);
      }
      const int rowoff = WS_HF + (4 * pn + t) * 49152 + r * 16;
      __builtin_amdgcn_raw_buffer_store_b128(v4u{o[0], o[1], o[2], o[3]}, rs, rowoff + kp * 1024 + g * 256, 0, AUX_SC1);
      __builtin_amdgcn_raw_buffer_store_b64(v2u{o[4], o[5]}, rs, rowoff + ksg * 1024 + (2 * wo + (g >> 1)) * 256 + (g & 1) * 8, 0, AUX_SC1);
    }
    publish(ctr + (64 + pn) * 64, ctr, false);
  }
  wait_ge(ctr + (64 + pn) * 64, 8u);
  {
    constexpr int WIN = 7;
    v4u af[WIN][4];
    int aoff[4];
#pragma unroll
    for (int t = 0; t < 4; ++t) aoff[t] = WS_HF + ((4 * pn + ((t + w) & 3)) * 48 + 12 * w) * 1024 + l16;
#pragma unroll
    for (int k = 0; k < WIN; ++k)
#pragma unroll
      for (int t = 0; t < 4; ++t) af[k][t] = __builtin_amdgcn_raw_buffer_load_b128(rs, aoff[t] + k * 1024, 0, AUX_SC1);
    SB();
#pragma unroll
    for (int nt = 0; nt < 5; ++nt)
#pragma unroll
      for (int t = 0; t < 4; ++t) acc[nt][t] = f32x4{0.f, 0.f, 0.f, 0.f};
#pragma unroll
    for (int k = 0; k < 12; ++k) {
#pragma unroll
      for (int t = 0; t < 4; ++t) {
        const v4u cur = af[k % WIN][t];
#pragma unroll
        for (int nt = 0; nt < 5; ++nt) acc[nt][t] = mfma(w4f[k * 5 + nt], cur, acc[nt][t]);
        if (k + WIN < 12) af[k % WIN][t] = __builtin_amdgcn_raw_buffer_load_b128(rs, aoff[t] + (k + WIN) * 1024, 0, AUX_SC1);
      }
      SB();
    }
  }
  reduce_write(S, w, lane, acc);
  wg_barrier();
  reduce_read(S, w, lane, acc);
  {
    unsigned o[10];
#pragma unroll
    for (int nt = 0; nt < 5; ++nt) {
      o[2 * nt] = pk2(res[4 * nt] + acc[nt][0][0], res[4 * nt + 1] + acc[nt][0][1]);
      o[2 * nt + 1] = pk2(res[4 * nt + 2] + acc[nt][0][2], res[4 * nt + 3] + acc[nt][0][3]);
    }
    GAS char* op = (GAS char*)P.out + ((size_t)(64 * pn + 16 * w + r) * D + 80 * c) * 2;
    *(GAS v4u*)(op + (pb + 8 * g) * 2) = v4u{o[0], o[1], o[2], o[3]};
    *(GAS v4u*)(op + (pb + 32 + 8 * g) * 2) = v4u{o[4], o[5], o[6], o[7]};
    *(GAS v2u*)(op + (sb + 4 * g) * 2) = v2u{o[8], o[9]};
  }
}

extern "C" void kernel_launch(void* const* d_in, const int* in_sizes, int n_in, void* d_out, int out_size,
                              void* d_ws, size_t ws_size, hipStream_t stream) {
  Ptrs P;
  P.x = (const u16*)d_in[0]; P.are = (const u16*)d_in[1]; P.aim = (const u16*)d_in[2];
  P.bre = (const u16*)d_in[3]; P.bim = (const u16*)d_in[4]; P.cre = (const u16*)d_in[5];
  P.cim = (const u16*)d_in[6]; P.logdt = (const u16*)d_in[7]; P.dskip = (const u16*)d_in[8];
  P.w1 = (const u16*)d_in[10]; P.w2 = (const u16*)d_in[11]; P.w3 = (const u16*)d_in[12]; P.w4 = (const u16*)d_in[13];
  P.out = (u16*)d_out; P.ws = (char*)d_ws;
  (void)hipFuncSetAttribute((const void*)k_conv, hipFuncAttributeMaxDynamicSharedMemorySize, CV_LDS);
  (void)hipFuncSetAttribute((const void*)k_mlp, hipFuncAttributeMaxDynamicSharedMemorySize, ML_LDS);
  k_prep<<<PREP_LN + PREP_KG + PREP_PK, 256, PREP_LDS, stream>>>(P);
  k_conv<<<CV_BLOCKS + CV_CS, 256, CV_LDS, stream>>>(P);
  k_mlp<<<256, 256, ML_LDS, stream>>>(P);
}
```

```cpp
#include <hip/hip_runtime.h>
#include <stdint.h>

typedef unsigned short u16;
typedef unsigned v4u __attribute__((ext_vector_type(4)));
typedef unsigned v2u __attribute__((ext_vector_type(2)));
typedef float f32x4 __attribute__((ext_vector_type(4)));
typedef float f32x2 __attribute__((ext_vector_type(2)));
typedef short s16x4 __attribute__((ext_vector_type(4)));
typedef __bf16 bf16x2 __attribute__((ext_vector_type(2)));
typedef __bf16 bf16x8 __attribute__((ext_vector_type(8)));
#define GAS __attribute__((address_space(1)))
#define LAS __attribute__((address_space(3)))
typedef GAS unsigned gu32;
#define RLX_AGENT __ATOMIC_RELAXED, __HIP_MEMORY_SCOPE_AGENT
#define DEV __device__ __forceinline__
#define SB() __builtin_amdgcn_sched_barrier(0)

constexpr int L = 2048, D = 640, NS = 192, F = 1536;
constexpr float EPS = 1e-5f;
constexpr int RSTR = 2112;

constexpr int WS_CTRL = 0;
constexpr int GF_OFF = 32768;
constexpr int GSH = 64, GRP = 4;
constexpr int WS_ST2 = 163840;
constexpr int WS_UT = WS_ST2 + 131072;
constexpr int WS_XT = WS_UT + D * L * 2;
constexpr int WS_RK = WS_XT + 16384;
constexpr int WS_HT = WS_RK + D * RSTR * 2;
constexpr int WS_W1 = WS_HT + D * L * 2;
constexpr int WS_W2 = WS_W1 + D * D * 2;
constexpr int WS_W3 = WS_W2 + D * D * 2;
constexpr int WS_W4 = WS_W3 + D * F * 2;
constexpr int GS_REC = 1280;
constexpr int WS_GS = WS_W4 + D * F * 2;
constexpr int WS_X1 = WS_GS + 128 * 20 * GS_REC;
constexpr int WS_HF = WS_X1 + D * L * 2;
constexpr int WS_END = WS_HF + F * L * 2;

DEV float bflo(unsigned v) { return __uint_as_float(v << 16); }
DEV float bfhi(unsigned v) { return __uint_as_float(v & 0xffff0000u); }
DEV float bf2f(u16 v) { return __uint_as_float(((unsigned)v) << 16); }
DEV unsigned pk2(float lo, float hi) {
  f32x2 f = {lo, hi};
  return __builtin_bit_cast(unsigned, __builtin_convertvector(f, bf16x2));
}
DEV f32x4 mfma(v4u a, v4u b, f32x4 c) {
  return __builtin_amdgcn_mfma_f32_16x16x32_bf16(__builtin_bit_cast(bf16x8, a), __builtin_bit_cast(bf16x8, b), c, 0, 0, 0);
}
DEV v2u trread(int a) {
  return __builtin_bit_cast(v2u, __builtin_amdgcn_ds_read_tr16_b64_v4i16((LAS s16x4*)(size_t)(unsigned)a));
}
DEV v2u trread(LAS char* p) {
  return __builtin_bit_cast(v2u, __builtin_amdgcn_ds_read_tr16_b64_v4i16((LAS s16x4*)p));
}
DEV float dot2(unsigned a, unsigned b, float c) {
  return __builtin_amdgcn_fdot2_f32_bf16(__builtin_bit_cast(bf16x2, a), __builtin_bit_cast(bf16x2, b), c, false);
}
DEV void wave_sync() {
  __builtin_amdgcn_fence(__ATOMIC_RELEASE, "wavefront");
  __builtin_amdgcn_wave_barrier();
  __builtin_amdgcn_fence(__ATOMIC_ACQUIRE, "wavefront");
}
DEV void wg_barrier() {
  asm volatile("s_waitcnt lgkmcnt(0)" ::: "memory");
  __builtin_amdgcn_s_barrier();
  asm volatile("" ::: "memory");
}
struct Ptrs {
  const u16 *x, *are, *aim, *bre, *bim, *cre, *cim, *logdt, *dskip, *w1, *w2, *w3, *w4;
  u16* out;
  char* ws;
};

#define CODE_ADDR(sym) ({ unsigned lo_, hi_; \
  asm volatile("s_getpc_b64 s[20:21]\n\ts_add_u32 %0, s20, " sym "@rel32@lo+4\n\ts_addc_u32 %1, s21, " sym "@rel32@hi+12" \
               : "=s"(lo_), "=s"(hi_) :: "scc", "s20", "s21"); ((unsigned long long)hi_ << 32) | lo_; })

constexpr int PREP_LN = 128, PREP_KG = 640, PREP_PK = 256;
constexpr int PREP_LDS = 25600;
#define STB(v, off) __builtin_amdgcn_raw_buffer_store_b128((v), rs, (off), 0, 16)
typedef __amdgpu_buffer_rsrc_t rsrc_t;

template <int C> DEV float dpp_add(float v) {
  return v + __uint_as_float(__builtin_amdgcn_update_dpp(0u, __float_as_uint(v), C, 0xf, 0xf, true));
}
DEV float sum16(float v) {
  v = dpp_add<0xB1>(v);
  v = dpp_add<0x4E>(v);
  v = dpp_add<0x141>(v);
  return dpp_add<0x140>(v);
}

DEV void prep_ln0(const Ptrs& P, rsrc_t rs, LAS char* S, int b) {
  const int tid = threadIdx.x, lane = tid & 63, w = tid >> 6;
  const int row = tid >> 4, j = tid & 15;
  const GAS v4u* xp = (const GAS v4u*)(P.x + (size_t)(b * 16 + row) * D) + j;
  v4u xv[5];
#pragma unroll
  for (int i = 0; i < 5; ++i) xv[i] = __builtin_nontemporal_load(xp + 16 * i);
  if (b >= 8 && b < 48) STB((v4u{0u, 0u, 0u, 0u}), WS_CTRL + ((b - 8) * 256 + tid) * 16);
  constexpr int STR = 1296;
  const int r = lane & 15, g = lane >> 4, qq = r >> 2, pp = lane & 3;
  LAS char* wp = S + row * STR + j * 16;
  LAS char* rp = S + (8 * (g & 1) + qq) * STR + (w * 160 + 16 * (g >> 1)) * 2 + pp * 8;
  const int go = b * 20480 + (w * 160 + 16 * (g >> 1) + r) * 32 + (g & 1) * 16;
  float s = 0.f, q = 0.f, s2 = 0.f, q2 = 0.f;
#pragma unroll
  for (int i = 0; i < 5; ++i)
#pragma unroll
    for (int e = 0; e < 4; e += 2) {
      s = dot2(xv[i][e], 0x3f803f80u, s); q = dot2(xv[i][e], xv[i][e], q);
      s2 = dot2(xv[i][e + 1], 0x3f803f80u, s2); q2 = dot2(xv[i][e + 1], xv[i][e + 1], q2);
    }
  s = sum16(s + s2); q = sum16(q + q2);
  const float m = s * (1.0f / D);
  const float ve = fmaxf(q * (1.0f / D) - m * m, 0.f) + EPS, rstd = __builtin_amdgcn_rsqf(ve);
  const float mr = -m * rstd;
#pragma unroll
  for (int i = 0; i < 5; ++i) {
#pragma unroll
    for (int e = 0; e < 4; ++e)
      xv[i][e] = pk2(__builtin_fmaf(bflo(xv[i][e]), rstd, mr), __builtin_fmaf(bfhi(xv[i][e]), rstd, mr));
    *(LAS v4u*)(wp + i * 256) = xv[i];
  }
  if (j < 2) *(LAS float*)(S + 20736 + j * 64 + row * 4) = j ? m : ve * rstd;
  wg_barrier();
#pragma unroll
  for (int i = 0; i < 5; ++i) {
    v2u lo = trread(rp + i * 64), hi = trread(rp + i * 64 + 4 * STR);
    STB((v4u{lo[0], lo[1], hi[0], hi[1]}), WS_UT + go + i * 1024);
  }
  if (tid < 8) STB(*(LAS v4u*)(S + 20736 + tid * 16), WS_XT + b * 128 + tid * 16);
  if (b < 3) {
    const int i = b * 256 + tid, rec = i >> 4;
    STB((v4u{0u, 0u, 0u, 0u}), i < 640 ? WS_GS + (rec < 20 ? rec : 127 * 20 + rec - 20) * GS_REC + 1024 + (i & 15) * 16 : WS_END);
  }
}

DEV int nmap80(int odd, int nt, int p) {
  return nt < 4 ? (odd ? 16 : 0) + 32 * (nt >> 1) + 8 * p + 4 * (nt & 1) : (odd ? 0 : 64) + 4 * p;
}
DEV int nmap48(int odd, int nt, int p) {
  return nt < 2 ? (odd ? 16 : 0) + 8 * p + 4 * nt : (odd ? 0 : 32) + 4 * p;
}

DEV void pack640_load(const u16* W, int uu, v4u (&v)[5]) {
  const int tid = threadIdx.x;
  const GAS v4u* src = (const GAS v4u*)(W + (size_t)(32 * (uu >> 1) + (tid >> 3)) * D + 320 * (uu & 1)) + (tid & 7);
#pragma unroll
  for (int i = 0; i < 5; ++i) v[i] = __builtin_nontemporal_load(src + 8 * i);
}
DEV void pack640_write(LAS char* S, v4u (&v)[5]) {
  const int tid = threadIdx.x;
#pragma unroll
  for (int i = 0; i < 5; ++i) *(LAS v4u*)(S + (tid >> 3) * 656 + ((tid & 7) + 8 * i) * 16) = v[i];
}
DEV void pack640_store(rsrc_t rs, LAS char* S, int dstoff, int KS, int uu) {
  const int lane = threadIdx.x & 63, w = threadIdx.x >> 6;
  const int g = lane >> 4, qq = (lane & 15) >> 2, pp = lane & 3;
  const int c = 4 * (uu & 1) + w, ks = uu >> 1;
#pragma unroll
  for (int nt = 0; nt < 5; ++nt) {
    LAS char* a = S + (8 * g + qq) * 656 + (80 * w + nmap80(c & 1, nt, pp)) * 2;
    v2u lo = trread(a), hi = trread(a + 4 * 656);
    STB((v4u{lo[0], lo[1], hi[0], hi[1]}), dstoff + (((c * KS + ks) * 5 + nt) * 64 + lane) * 16);
  }
}

DEV void pack_w3(const Ptrs& P, rsrc_t rs, LAS char* S, int u3) {
  const int tid = threadIdx.x, lane = tid & 63, w = tid >> 6;
  v4u v3[6];
  const GAS v4u* src = (const GAS v4u*)(P.w3 + (size_t)(32 * (u3 >> 2) + (tid >> 3)) * F + 384 * (u3 & 3)) + (tid & 7);
#pragma unroll
  for (int i = 0; i < 6; ++i) v3[i] = __builtin_nontemporal_load(src + 8 * i);
#pragma unroll
  for (int i = 0; i < 6; ++i) *(LAS v4u*)(S + (tid >> 3) * 784 + ((tid & 7) + 8 * i) * 16) = v3[i];
  wg_barrier();
  const int g = lane >> 4, qq = (lane & 15) >> 2, pp = lane & 3;
  const int ks = u3 >> 2, qt = u3 & 3;
#pragma unroll
  for (int h = 0; h < 2; ++h) {
    const int wg = 2 * w + h;
    const int cw = 8 * qt + wg;
#pragma unroll
    for (int nt = 0; nt < 3; ++nt) {
      LAS char* a = S + (8 * g + qq) * 784 + (48 * wg + nmap48(h, nt, pp)) * 2;
      v2u lo = trread(a), hi = trread(a + 4 * 784);
      STB((v4u{lo[0], lo[1], hi[0], hi[1]}), WS_W3 + (((cw * 20 + ks) * 3 + nt) * 64 + lane) * 16);
    }
  }
}

DEV f32x2 cmul(f32x2 a, f32x2 b) {
  f32x2 t, r;
  asm("v_pk_mul_f32 %0, %1, %2 op_sel_hi:[1,0]" : "=v"(t) : "v"(a), "v"(b));
  asm("v_pk_fma_f32 %0, %1, %2, %3 op_sel:[1,1,0] op_sel_hi:[0,1,1] neg_lo:[1,0,0]" : "=v"(r) : "v"(a), "v"(b), "v"(t));
  return r;
}
DEV f32x2 cexpl(float a2, float br, float l) {
  const float e = __builtin_amdgcn_exp2f(a2 * l);
  const float ph = __builtin_amdgcn_fractf(br * l);
  return f32x2{e * __builtin_amdgcn_cosf(ph), e * __builtin_amdgcn_sinf(ph)};
}
DEV unsigned pkc(f32x2 v) { return pk2(v[0], v[1]); }

DEV int padx(int i) { return i + (i >> 5); }
struct KgIn { u16 are, aim, bre, bim, cre, cim, ldt; };
DEV void prep_kgen(const Ptrs& P, rsrc_t rs, LAS char* S, int d, const KgIn& in) {
  const int tid = threadIdx.x, lane = tid & 63, w = __builtin_amdgcn_readfirstlane(tid >> 6);
  constexpr int TSTR = 288;
  constexpr int TB = 16 * TSTR;
  const int part = (int)(size_t)S + 3 * TB;
  unsigned touch2 = 0;
  int ix[8];
#define KG_IDX _Pragma("unroll") for (int e = 0; e < 4; ++e) { \
      const int l0 = 2047 - 8 * tid - 2 * e, l1 = l0 - 1; \
      const unsigned bb = pk2((float)l0, (float)l1); \
      ix[2 * e] = part + 4 * padx(l0 < 128 ? l0 : (int)(bb & 0xffffu) - 0x4300 + 128); \
      ix[2 * e + 1] = part + 4 * padx(l1 < 128 ? l1 : (int)(bb >> 16) - 0x4300 + 128); \
      asm volatile("" : "+v"(ix[2 * e]), "+v"(ix[2 * e + 1])); }
  if (__builtin_expect(w < 3, 1)) {
    KG_IDX
    int sc0, sc1, sc2, s1, s2;
    {
      const int r = lane & 15, g = lane >> 4;
      int b1, b2;
      if (r < 8) { b1 = 256 + r; s1 = 8; b2 = 512 + 32 * (r >> 1) + (r & 1); s2 = 2; }
      else { b1 = 384 + 64 * ((r - 8) >> 2) + (r & 3); s1 = 4; b2 = (r == 8) ? 640 : 672; s2 = 1; }
      sc0 = part + w * 3072 + (66 * g + r) * 4; sc1 = part + w * 3072 + padx(b1 + s1 * 4 * g) * 4; sc2 = part + w * 3072 + padx(b2 + s2 * 4 * g) * 4;
      s1 *= 4; s2 *= 4;
      asm volatile("" : "+v"(sc0), "+v"(sc1), "+v"(sc2), "+v"(s1), "+v"(s2));
    }
    SB();
    const float are = bf2f(in.are), aim = bf2f(in.aim), bre = bf2f(in.bre), bim = bf2f(in.bim);
    const float cre = bf2f(in.cre), cim = bf2f(in.cim);
    const float dt = bflo(pk2(expf(bf2f(in.ldt)), 0.f));
    const float a2 = dt * are * 1.4426950408889634f, br = dt * aim * 0.15915494309189535f;
    LAS char* T = S + w * TB;
    LAS char* wr = T + lane * 4;
    LAS char* rd = T + (lane & 15) * TSTR + (lane >> 4) * 16;
    const f32x2 e1 = cexpl(a2, br, 1.f), e256 = cexpl(a2, br, 256.f);
    {
      const f32x2 Wc = cmul(f32x2{cre, -cim}, f32x2{dt * bre, -dt * bim});
      const f32x2 e16 = cexpl(a2, -br, 16.f);
      const f32x2 e32 = cmul(e16, e16), e64 = cmul(e32, e32);
      f32x2 h0 = Wc, h1 = cmul(Wc, e16), h2 = cmul(Wc, e32), h3 = cmul(h1, e32);
#pragma unroll
      for (int a = 0; a < 16; a += 4) {
        *(LAS unsigned*)(wr + a * TSTR) = pkc(h0);
        *(LAS unsigned*)(wr + (a + 1) * TSTR) = pkc(h1);
        *(LAS unsigned*)(wr + (a + 2) * TSTR) = pkc(h2);
        *(LAS unsigned*)(wr + (a + 3) * TSTR) = pkc(h3);
        if (a < 12) { h0 = cmul(h0, e64); h1 = cmul(h1, e64); h2 = cmul(h2, e64); h3 = cmul(h3, e64); }
      }
    }
    wave_sync();
    v4u af[4];
#pragma unroll
    for (int k = 0; k < 4; ++k) af[k] = *(LAS v4u*)(rd + k * 64);
    wave_sync();
    f32x4 acc[3];
    f32x2 ev[8];
    {
      const f32x2 e2 = cmul(e1, e1);
      ev[0] = f32x2{1.f, 0.f}; ev[1] = e2; ev[2] = cmul(e2, e2); ev[3] = cmul(ev[2], e2);
      ev[4] = cmul(ev[2], ev[2]); ev[5] = cmul(ev[4], e2); ev[6] = cmul(ev[4], ev[2]); ev[7] = cmul(ev[4], ev[3]);
#pragma unroll
      for (int c = 0; c < 8; ++c) {
        const f32x2 o = c ? cmul(ev[c], e1) : e1;
        *(LAS unsigned*)(wr + (2 * c) * TSTR) = pkc(ev[c]);
        *(LAS unsigned*)(wr + (2 * c + 1) * TSTR) = pkc(o);
      }
    }
    v4u bf[3][4];
#define KG_MMA(T_)                                                            \
  {                                                                           \
    wave_sync();                                                              \
    _Pragma("unroll") for (int k = 0; k < 4; ++k) bf[T_][k] = *(LAS v4u*)(rd + k * 64); \
    wave_sync();                                                              \
  }
    KG_MMA(0)
    const f32x2 e512 = cmul(e256, e256), e768 = cmul(e512, e256), e1024 = cmul(e512, e512);
#pragma unroll
    for (int c = 0; c < 8; ++c) *(LAS unsigned*)(wr + c * TSTR) = pkc(c ? cmul(e256, ev[c]) : e256);
#pragma unroll
    for (int c = 0; c < 4; ++c) {
      *(LAS unsigned*)(wr + (8 + c) * TSTR) = pkc(c ? cmul(e512, ev[2 * c]) : e512);
      *(LAS unsigned*)(wr + (12 + c) * TSTR) = pkc(c ? cmul(e768, ev[2 * c]) : e768);
    }
    KG_MMA(1)
    {
      f32x2 bs[4] = {e1024, cmul(e1024, e256), cmul(e1024, e512), cmul(e1024, e768)};
#pragma unroll
      for (int c = 0; c < 4; ++c) {
        *(LAS unsigned*)(wr + (2 * c) * TSTR) = pkc(bs[c]);
        *(LAS unsigned*)(wr + (2 * c + 1) * TSTR) = pkc(cmul(bs[c], ev[4]));
      }
      *(LAS unsigned*)(wr + 8 * TSTR) = pkc(cmul(e1024, e1024));
    }
    KG_MMA(2)
#undef KG_MMA
    asm volatile("" : "+v"(af[0]), "+v"(af[1]), "+v"(af[2]), "+v"(af[3]));
    SB();
#pragma unroll
    for (int k = 0; k < 4; ++k)
#pragma unroll
      for (int t = 0; t < 3; ++t) acc[t] = mfma(af[k], bf[t][k], k ? acc[t] : f32x4{0.f, 0.f, 0.f, 0.f});
#pragma unroll
    for (int i = 0; i < 4; ++i) {
      *(LAS float*)(size_t)(unsigned)(sc0 + 64 * i + 4 * (i >> 1)) = acc[0][i];
      *(LAS float*)(size_t)(unsigned)(sc1 + s1 * i) = acc[1][i];
      *(LAS float*)(size_t)(unsigned)(sc2 + s2 * i) = acc[2][i];
    }
  } else {
    const unsigned dsv = (unsigned)in.ldt << 16;
    KG_IDX
    if (lane < 8) STB((v4u{lane == 1 ? dsv : 0u, 0u, 0u, 0u}), WS_RK + d * RSTR * 2 + 4096 + lane * 16);
    if (blockIdx.x >= PREP_LN && blockIdx.x < PREP_LN + 64) {
      const unsigned long long c0 = CODE_ADDR("_Z5k_mlp4Ptrs") + ((((int)blockIdx.x - PREP_LN) >> 3) * 64 + lane) * 128, c1 = CODE_ADDR("_Z5k_end4Ptrs");
      if (c0 < c1) touch2 = *(const GAS unsigned*)c0;
    }
  }
  wg_barrier();
  {
    float v[8];
#pragma unroll
    for (int e = 0; e < 8; ++e)
      v[e] = (*(LAS float*)(size_t)(unsigned)ix[e] + *(LAS float*)(size_t)(unsigned)(ix[e] + 3072)) + *(LAS float*)(size_t)(unsigned)(ix[e] + 6144);
    STB((v4u{pk2(v[0], v[1]), pk2(v[2], v[3]), pk2(v[4], v[5]), pk2(v[6], v[7])}), WS_RK + d * RSTR * 2 + tid * 16);
  }
  asm volatile("" ::"v"(touch2));
}

__global__ void __launch_bounds__(256, 4) __attribute__((aligned(4096))) k_prep(Ptrs P) {
  extern __shared__ __attribute__((aligned(16))) char smem[];
  LAS char* S = (LAS char*)smem;
  const rsrc_t rs = __builtin_amdgcn_make_buffer_rsrc((void*)P.ws, 0, WS_END, 0x00020000);
  const int b = blockIdx.x;
  KgIn in;
  {
    const int dd = b < PREP_KG ? b : 0, tid = threadIdx.x;
    const int i = dd * NS + (tid < NS ? tid : 0);
    in.are = P.are[i]; in.aim = P.aim[i]; in.bre = P.bre[i]; in.bim = P.bim[i]; in.cre = P.cre[i]; in.cim = P.cim[i];
    in.ldt = (tid < NS ? P.logdt : P.dskip)[dd];
    SB();
    if (b < 16 && tid >= NS) {
      unsigned t_;
      asm volatile("global_load_dword %0, %1, %2\n\ts_waitcnt vmcnt(0)" : "=&v"(t_) : "v"((tid - NS) * 128), "s"(CODE_ADDR("_Z6k_prep4Ptrs")) : "memory");
    }
  }
  if (__builtin_expect(b < PREP_KG, 1)) { prep_kgen(P, rs, S, b, in); }
  else if (b < PREP_LN + PREP_KG) { prep_ln0(P, rs, S, b - PREP_KG); }
  else {
    const int u = b - PREP_LN - PREP_KG;
    if (u < 176) {
      v4u v[5];
      const int uu = u < 40 ? u : u < 80 ? u - 40 : u - 80;
      pack640_load(u < 40 ? P.w1 : u < 80 ? P.w2 : P.w4, uu, v);
      pack640_write(S, v);
      wg_barrier();
      pack640_store(rs, S, u < 40 ? WS_W1 : u < 80 ? WS_W2 : WS_W4, u < 80 ? 20 : 48, uu);
    } else pack_w3(P, rs, S, u - 176);

  }
}

constexpr int CV_KSTR = 4416, CV_USTR = 5152, CV_WAVE = 4 * CV_KSTR + 2 * CV_USTR + 96;
constexpr int CV_XL = 4 * CV_WAVE;
constexpr int CV_BLOCKS = 160;
constexpr int ML_TILE = 0;
constexpr int ML_RED = 81920;
constexpr int ML_STAT = ML_RED + 61440;
constexpr int ML_MR = ML_STAT + 5120;
constexpr int ML_DUM = ML_MR + 1024;
constexpr int ML_CS = ML_DUM + 1024;
constexpr int ML_LDS = ML_CS + 1280;

DEV float sum_g(float a) {
  auto p = __builtin_amdgcn_permlane16_swap(__float_as_uint(a), __float_as_uint(a), false, false);
  a = __uint_as_float(p[0]) + __uint_as_float(p[1]);
  auto q = __builtin_amdgcn_permlane32_swap(__float_as_uint(a), __float_as_uint(a), false, false);
  return __uint_as_float(q[0]) + __uint_as_float(q[1]);
}
struct GK { f32x4 nm, k1, k2, ir; };
DEV GK gelu_k(float m, float rr) {
  const float k1 = -2.302208198f * rr, k2 = -0.1029432397f * rr * rr * rr, ir = __builtin_amdgcn_rcpf(rr);
  return GK{f32x4{-m, -m, -m, -m}, f32x4{k1, k1, k1, k1}, f32x4{k2, k2, k2, k2}, f32x4{ir, ir, ir, ir}};
}
template <int N> DEV void ln_gelu_n(const f32x4* a, const f32x4* cs, const GK& k, v2u* o) {
  f32x4 t[N], u[N];
#pragma unroll
  for (int n = 0; n < N; ++n) t[n] = __builtin_elementwise_fma(cs[n], k.nm, a[n]);
  SB();
#pragma unroll
  for (int n = 0; n < N; ++n) u[n] = t[n] * t[n];
  SB();
#pragma unroll
  for (int n = 0; n < N; ++n) u[n] = __builtin_elementwise_fma(u[n], k.k2, k.k1);
  SB();
#pragma unroll
  for (int n = 0; n < N; ++n) u[n] = u[n] * t[n];
  SB();
#pragma unroll
  for (int n = 0; n < N; ++n)
#pragma unroll
    for (int i = 0; i < 4; ++i) u[n][i] = __builtin_amdgcn_exp2f(u[n][i]);
  SB();
#pragma unroll
  for (int n = 0; n < N; ++n) u[n] = __builtin_elementwise_fma(u[n], k.ir, k.ir);
  SB();
#pragma unroll
  for (int n = 0; n < N; ++n)
#pragma unroll
    for (int i = 0; i < 4; ++i) u[n][i] = __builtin_amdgcn_rcpf(u[n][i]);
  SB();
#pragma unroll
  for (int n = 0; n < N; ++n) u[n] = u[n] * t[n];
  SB();
#pragma unroll
  for (int n = 0; n < N; ++n) o[n] = v2u{pk2(u[n][0], u[n][1]), pk2(u[n][2], u[n][3])};
}
DEV void wait_all(gu32* c) {
  unsigned spins = 0;
#pragma unroll 1
  while (!__all(__hip_atomic_load(c, RLX_AGENT) != 0u) && ++spins < (1u << 17)) __builtin_amdgcn_s_sleep(1);
  __builtin_amdgcn_fence(__ATOMIC_ACQUIRE, "wavefront");
}
DEV void wait_blk(gu32* c) {
  if (threadIdx.x < 16) wait_all(c);
  wg_barrier();
}
DEV void wait_gridw(gu32* c) {
  unsigned spins = 0;
#pragma unroll 1
  while (!__all(__hip_atomic_load(c, RLX_AGENT) >= 5u) && ++spins < (1u << 17)) __builtin_amdgcn_s_sleep(1);
  __builtin_amdgcn_fence(__ATOMIC_ACQUIRE, "wavefront");
}
DEV void dma_plain(rsrc_t rs, int lds, int voff, int soff) {
  asm volatile("s_mov_b32 m0, %0\n\ts_nop 0\n\tbuffer_load_dwordx4 %1, %2, %3 offen lds" ::"s"(lds), "v"(voff), "s"(rs), "s"(soff) : "memory");
}
DEV void dma_sc1(rsrc_t rs, int lds, int voff, int soff) {
  asm volatile("s_mov_b32 m0, %0\n\ts_nop 0\n\tbuffer_load_dwordx4 %1, %2, %3 offen sc1 lds" ::"s"(lds), "v"(voff), "s"(rs), "s"(soff) : "memory");
}
DEV void p3_stat(int dst, f32x4 v) {
#pragma unroll
  for (int i = 0; i < 4; ++i) v[i] = sum_g(dpp_add<0x128>(v[i]));
#pragma unroll
  for (int h = 0; h < 2; ++h) {
    const float m = v[2 * h] * (1.0f / D);
    const float ve = fmaxf(v[2 * h + 1] * (1.0f / D) - m * m, 0.f) + EPS, rr = __builtin_amdgcn_rsqf(ve);
    *(LAS f32x4*)(size_t)(unsigned)(dst + 16 * h) = f32x4{-m, -2.302208198f * rr, -0.1029432397f * rr * rr * rr, ve * rr};
  }
}
#define LDA(d, vo, so, im) asm volatile("buffer_load_dwordx4 %0, %1, %2, %3 offen offset:" #im : "=a"(d) : "v"(vo), "s"(rs), "s"(so) : "memory")
#define LDV(d, vo, so, im) asm volatile("buffer_load_dwordx4 %0, %1, %2, %3 offen offset:" #im : "=v"(d) : "v"(vo), "s"(rs), "s"(so) : "memory")
#define LDVS(d, vo, so, im) asm volatile("buffer_load_dwordx4 %0, %1, %2, %3 offen offset:" #im " sc1" : "=v"(d) : "v"(vo), "s"(rs), "s"(so) : "memory")
#define LDV2(d, vo, so, im) asm volatile("buffer_load_dwordx2 %0, %1, %2, %3 offen offset:" #im : "=v"(d) : "v"(vo), "s"(rs), "s"(so) : "memory")
#define ST4(a, b, off) __builtin_amdgcn_raw_buffer_store_b128(v4u{(a)[0], (a)[1], (b)[0], (b)[1]}, rs, (off), 0, 16)
#define ST2(a, off) __builtin_amdgcn_raw_buffer_store_b64((a), rs, (off), 0, 16)
#define LDV1(d, vo, so, im) asm volatile("buffer_load_dword %0, %1, %2, %3 offen offset:" #im : "=v"(d) : "v"(vo), "s"(rs), "s"(so) : "memory")
#define RD2(d0, d1, a0, a1) asm volatile("v_accvgpr_read_b32 %0, %2\n\tv_accvgpr_read_b32 %1, %3" : "=&v"(d0), "=v"(d1) : "a"(a0), "a"(a1))
#define ADD2(d0, d1, x0, x1) asm volatile("v_add_f32 %0, %0, %2\n\tv_add_f32 %1, %1, %3" : "+v"(d0), "+v"(d1) : "v"(x0), "v"(x1))
#define MUL2(d0, d1, x0, x1, y0, y1) asm volatile("v_mul_f32 %0, %2, %4\n\tv_mul_f32 %1, %3, %5" : "=&v"(d0), "=v"(d1) : "v"(x0), "v"(x1), "v"(y0), "v"(y1))
#define FMA2(d0, d1, k, c) asm volatile("v_fma_f32 %0, %0, %2, %3\n\tv_fma_f32 %1, %1, %2, %3" : "+v"(d0), "+v"(d1) : "v"(k), "v"(c))
#define ADDK2(d0, d1, k) asm volatile("v_add_f32 %0, %0, %2\n\tv_add_f32 %1, %1, %2" : "+v"(d0), "+v"(d1) : "v"(k))
#define FMAC2B(d0, d1, x0, x1, y0, y1) asm volatile("v_fmac_f32 %0, %2, %4\n\tv_fmac_f32 %1, %3, %5" : "+v"(d0), "+v"(d1) : "v"(x0), "v"(x1), "v"(y0), "v"(y1))
#define FMA2N(d0, d1, x0, x1, k, c0, c1) asm volatile("v_fma_f32 %0, %2, %4, %5\n\tv_fma_f32 %1, %3, %4, %6" : "=&v"(d0), "=v"(d1) : "v"(x0), "v"(x1), "v"(k), "v"(c0), "v"(c1))
#define MMA(d, a, b) asm volatile("v_mfma_f32_16x16x32_bf16 %0, %1, %2, %0" : "+v"(d) : "a"(a), "v"(b))
#define MMV(d, a, b) asm volatile("v_mfma_f32_16x16x32_bf16 %0, %1, %2, %0" : "+v"(d) : "v"(a), "v"(b))
#define MM0A(d, a, b) asm volatile("v_mfma_f32_16x16x32_bf16 %0, %1, %2, 0" : "=&v"(d) : "a"(a), "v"(b))
#define MM0V(d, a, b) asm volatile("v_mfma_f32_16x16x32_bf16 %0, %1, %2, 0" : "=&v"(d) : "v"(a), "v"(b))
#define EXP1(d) asm volatile("v_exp_f32 %0, %0" : "+v"(d))
#define RCP1(d) asm volatile("v_rcp_f32 %0, %0" : "+v"(d))
#define UNP2(d0, d1, x) asm volatile("v_lshlrev_b32 %0, 16, %2\n\tv_and_b32 %1, 0xffff0000, %2" : "=&v"(d0), "=v"(d1) : "v"(x))
#define CVT2(d0, d1, s) asm volatile("v_cvt_pk_bf16_f32 %0, %2, %3\n\tv_cvt_pk_bf16_f32 %1, %4, %5" : "=&v"(d0), "=v"(d1) : "v"((s)[0]), "v"((s)[1]), "v"((s)[2]), "v"((s)[3]))
#define SIGNAL(e) __builtin_amdgcn_raw_ptr_buffer_atomic_add_i32(1, rs, sgo, (e) * 32768, 0)
#define POLL(e) asm volatile("buffer_load_dword %0, %1, %2, %3 offen sc1" : "=v"(fv) : "v"(zr2), "s"(rs), "s"((e) * 32768) : "memory")
#define CHECK(e) if (!__all(fv != 0u)) wait_all(ctr + (e) * 8192 + (zr2 >> 2)); SB();
#define PAD(n) asm volatile("cpad_" #n ":\n\ts_cbranch_execz ctramp")
#define TIE(x) asm volatile("" : "+v"(x))

__global__ void __launch_bounds__(256, 1) __attribute__((aligned(4096))) k_mlp(Ptrs P) {
  extern __shared__ __attribute__((aligned(16))) char smem[];
  LAS char* S = (LAS char*)smem;
  const int lane = threadIdx.x & 63, w = __builtin_amdgcn_readfirstlane(threadIdx.x >> 6);
  const int bx = blockIdx.x, pn = (bx & 7) * 4 + (bx >> 6), c = (bx >> 3) & 7;
  const int odd = c & 1;
  const rsrc_t rs = __builtin_amdgcn_make_buffer_rsrc((void*)P.ws, 0, WS_END, 0x00020000);
  gu32* ctr = (gu32*)(P.ws + WS_CTRL);
  const int l16 = lane * 16;
  const int ldsb = (int)(size_t)S;
  const int pb = odd ? 16 : 0, sb = odd ? 0 : 64;
  const f32x4 Z4 = {0.f, 0.f, 0.f, 0.f};
  const v4u ones = {0x3f803f80u, 0x3f803f80u, 0x3f803f80u, 0x3f803f80u};

  const int hb = WS_HT + pn * 81920 + w * 20480, xb = WS_X1 + pn * 81920 + w * 5120;
  const int w1b = WS_W1 + (c * 20 + 5 * w) * 5120, w2b = WS_W2 + (c * 20 + 5 * w) * 5120;
  const int w3b = WS_W3 + (c * 4 + w) * 61440, w4b = WS_W4 + (c * 48 + 12 * w) * 5120;
  int ab2[4], ab4[4];
#pragma unroll
  for (int t = 0; t < 4; ++t) {
    ab2[t] = WS_GS + ((4 * pn + ((t + 1 + w) & 3)) * 20 + 5 * w) * GS_REC;
    ab4[t] = WS_HF + ((4 * pn + (t ^ 1 ^ (w & 1))) * 48 + 12 * w) * 1024;
  }
  const int wo = w & 1, n0 = 192 * c + 48 * w;
  const int gsh = bx < CV_BLOCKS ? (bx / 40) * 8 + (bx & 7) : bx < CV_BLOCKS + 80 ? 32 + ((bx - CV_BLOCKS) / 20) * 8 + (bx & 3) : 63;
  int csr, csw, sto, f_p, f_s, g_p, g_s, x_p, x_s, h_p, h_s, hro[5], rp[4], redw, redr[3], stq, sts, strd, xr, st2o, o_o, a2v, a2w, g_hp, g_hs, sgo, zro, zr2, fl1, redw4, str0, str1, str2, str3, stq0, stq1, stq2, stq3;
#define LANEVARS int ln_ = lane; TIE(ln_); const int r = ln_ & 15, g = ln_ >> 4, q = r >> 2, p = ln_ & 3; (void)q; (void)p; (void)g;
#define ADDR_A { LANEVARS \
    _Pragma("unroll") for (int t = 0; t < 4; ++t) { \
      rp[t] = ldsb + ML_TILE + w * 20480 + (8 * g + q) * 128 + ((((t + w) & 3) ^ ((q >> 1) | ((g & 1) << 1))) << 5) + 8 * p; TIE(rp[t]); } \
    redw = ldsb + ML_RED + w * 15360 + ln_ * 16; TIE(redw); \
    stq = ldsb + (g == q ? ML_STAT + w * 1280 + r * 16 : ML_DUM); TIE(stq); \
    sts = ldsb + ML_STAT + w * 1280 + 1024 + r * 4; TIE(sts); \
    csr = ldsb + ML_CS + g * 16; csw = csr + w * 320; TIE(csr); TIE(csw); \
    }
#define ADDR_B { LANEVARS \
    _Pragma("unroll") for (int tau = 1; tau < 4; ++tau) { \
      redr[tau - 1] = ldsb + ML_RED + ((w - tau) & 3) * 15360 + (tau - 1) * 5120 + ln_ * 16; TIE(redr[tau - 1]); } \
    str0 = ldsb + ML_STAT + 1024 + r * 4 + (w & 3) * 64; str1 = ldsb + ML_STAT + 2304 + r * 4 + ((w - 1) & 3) * 64; \
    str2 = ldsb + ML_STAT + 3584 + r * 4 + ((w - 2) & 3) * 64; str3 = ldsb + ML_STAT + 4864 + r * 4 + ((w - 3) & 3) * 64; \
    stq0 = ldsb + ML_STAT + r * 16 + p * 4 + (w & 3) * 256; stq1 = ldsb + ML_STAT + 1280 + r * 16 + p * 4 + ((w - 1) & 3) * 256; \
    stq2 = ldsb + ML_STAT + 2560 + r * 16 + p * 4 + ((w - 2) & 3) * 256; stq3 = ldsb + ML_STAT + 3840 + r * 16 + p * 4 + ((w - 3) & 3) * 256; \
    TIE(str0); TIE(str1); TIE(str2); TIE(str3); TIE(stq0); TIE(stq1); TIE(stq2); TIE(stq3); \
    f_p = ((80 * c + pb) >> 5) * 1024 + g * 256; \
    f_s = ((80 * c + sb) >> 5) * 1024 + (2 * odd + (g >> 1)) * 256 + (g & 1) * 8; \
    { const int rp_ = WS_GS + ((4 * pn + w) * 20 + ((80 * c + pb) >> 5)) * GS_REC, rs_ = WS_GS + ((4 * pn + w) * 20 + ((80 * c + sb) >> 5)) * GS_REC; \
      g_p = rp_ + g * 256 + r * 16; g_s = rs_ + (2 * odd + (g >> 1)) * 256 + r * 16 + (g & 1) * 8; \
      const bool up = !(c & 2); const int nt_ = 4 * pn + w + (up ? 1 : -1), dl = (up ? 20 : -20) * GS_REC + 1024; \
      const int ia = up ? r - 14 : r, ib = up ? r - 12 : r; const bool okt = nt_ >= 0 && nt_ < 128; \
      g_hp = (okt && (unsigned)ia < 2u) ? rp_ + dl + g * 32 + ia * 16 : WS_END; \
      g_hs = (okt && (unsigned)ib < 4u) ? rs_ + dl + odd * 128 + (g >> 1) * 64 + ib * 16 + (g & 1) * 8 : WS_END; \
      TIE(g_p); TIE(g_s); TIE(g_hp); TIE(g_hs); } \
    sgo = threadIdx.x == 0 ? (pn * 8 + c) * 128 : WS_END; zro = (pn * 8 + (ln_ & 7)) * 128; TIE(sgo); TIE(zro); \
    x_p = WS_X1 + (4 * pn + w) * 20480 + r * 16 + f_p; x_s = WS_X1 + (4 * pn + w) * 20480 + r * 16 + f_s; TIE(x_p); TIE(x_s); }
#define ADDR_C { LANEVARS \
    hro[0] = ldsb + ML_TILE + (80 * c + pb + 8 * g + q) * 128 + ((w ^ ((q >> 1) | ((g & 1) << 1))) << 5) + 8 * p; \
    hro[1] = hro[0] + 512; hro[2] = hro[0] + 4096; hro[3] = hro[0] + 4608; \
    hro[4] = ldsb + ML_TILE + (80 * c + sb + 4 * g + q) * 128 + ((w ^ ((q >> 1) | (g & 2))) << 5) + 8 * p; \
    { const int rs_ = r - ((w & 1) ? -1 : 1) * (1 + (w >> 1)); const bool in_ = (unsigned)rs_ < 16u; \
      a2v = in_ ? g * 256 + rs_ * 16 : 1024 + g * 32 + (rs_ < 0 ? rs_ + 2 : rs_ - 16) * 16; \
      a2w = in_ ? g * 256 + rs_ * 16 : 1024 + g * 64 + (rs_ < 0 ? rs_ + 4 : rs_ - 16) * 16; TIE(a2v); TIE(a2w); } \
    { int np = (ln_ & 2) ? pn + ((w & 1) ? 1 : -1) : pn; np = np < 0 ? 0 : np > 31 ? 31 : np; fl1 = (np * 8 + 2 * w + (ln_ & 1)) * 32; TIE(fl1); } \
    st2o = g == 0 ? WS_ST2 + ((pn * 8 + c) * 64 + 16 * w + r) * 8 : WS_END; TIE(st2o); }
#define ADDR_D { LANEVARS \
    sto = ((pn * 8 + (ln_ >> 3)) * 64 + 16 * w) * 8 + (ln_ & 7) * 16; TIE(sto); \
    h_p = WS_HF + 4 * pn * 49152 + r * 16 + ((n0 + (wo ? 16 : 0)) >> 5) * 1024 + g * 256; TIE(h_p); \
    h_s = WS_HF + 4 * pn * 49152 + r * 16 + ((n0 + (wo ? 0 : 32)) >> 5) * 1024 + (2 * wo + (g >> 1)) * 256 + (g & 1) * 8; TIE(h_s); \
    xr = ldsb + ML_TILE + ln_ * 16; TIE(xr); \
    zr2 = (pn * 8 + 2 * w + (ln_ & 1)) * 128; TIE(zr2); \
    strd = ldsb + ML_MR + r * 16; TIE(strd); \
    sts = ldsb + (ln_ < 8 ? ML_MR + 256 * w + ln_ * 32 : ML_DUM); TIE(sts); }
#define ADDR_E { LANEVARS \
    o_o = ((64 * pn + 16 * w + r) * D + 80 * c) * 2; f_p = o_o + (pb + 8 * g) * 2; f_s = o_o + (sb + 4 * g) * 2; TIE(f_p); TIE(f_s); \
    redw4 = ldsb + w * 20480 + ln_ * 16; TIE(redw4); \
    redr[0] = ldsb + ((w & 1) ^ 1) * 20480 + (w & 2) * 5120 + ln_ * 16; redr[1] = ldsb + (w & 1) * 20480 + ((w & 2) + 1) * 5120 + ln_ * 16; TIE(redr[0]); TIE(redr[1]); }
#define CONV_VARS \
    const int r = lane & 15, g = lane >> 4, KB = ldsb + w * CV_WAVE, UB = KB + 4 * CV_KSTR; \
    const int koff = WS_RK + d * RSTR * 2 + l16; \
    v4u kv[4], uv[4], ta[2][8], tb[2][8]; v2u kn[4], uu; f32x4 cacc[8], cb[8]; float dsk[2]; \
    const v4u z = {0u, 0u, 0u, 0u};
#define CONV_ADDR_F \
    const int d = bx * 4 + w; const bool live = true; CONV_VARS \
    const int uoff = bx * 128 + (32 * w + (lane >> 3)) * 20480 + (lane & 7) * 16;
#define CONV_ADDR_E \
    const bool live = bx < CV_BLOCKS + 80; const int se = live ? bx - CV_BLOCKS : 79, d = se * 8 + 2 * w; CONV_VARS \
    const int uoff = se * 256 + (16 * w + (lane >> 4)) * 20480 + (lane & 15) * 16;
#define CONV_ZERO_F \
    LP(v4u, KB + ((lane >> 2) & 3) * CV_KSTR + 4096 + (lane & 3) * 16) = z; \
    LP(v4u, UB + (lane >> 5) * CV_USTR + (lane & 31) * 16) = z; \
    LP(v2u, UB + 512) = v2u{0u, 0u}; \
    int uw = ldsb + ((lane >> 1) & 3) * CV_WAVE + 4 * CV_KSTR + 512 + w * 1024 + (lane >> 3) * 32 + (lane & 1) * 16;
#define CONV_ZERO_E \
    LP(v4u, KB + ((lane >> 2) & 3) * CV_KSTR + ((lane >> 4) & 1) * 2112 + 2048 + (lane & 3) * 16) = z; \
    LP(v4u, UB + (lane >> 5) * CV_USTR + (lane & 31) * 16) = z; \
    LP(v4u, UB + (lane >> 5) * CV_USTR + 2576 + (lane & 31) * 16) = z; \
    LP(v2u, UB + (lane & 1) * 2576 + 512) = v2u{0u, 0u}; \
    int uw = ldsb + ((lane >> 2) & 3) * CV_WAVE + 4 * CV_KSTR + ((lane >> 1) & 1) * 2576 + 512 + (16 * w + (lane >> 4)) * 32 + (lane & 1) * 16;
#define CONV_COMMON \
    const int toff = 8 * g + 4 * (r & 1) + 32 * (r >> 1);    \
    int zero = 0; TIE(zero); const int dso = WS_RK + d * RSTR * 2 + 4112; \
    int pa = KB + (r & 3) * CV_KSTR + (24 + 8 * g - 8 * (r >> 2)) * 2; \
    int pbb = UB + (r & 1) * CV_USTR + (32 + 32 * (r >> 1) + 8 * g) * 2; \
    int xoff = ldsb + CV_XL + (toff >> 4) * 128 + (toff & 15) * 4; \
    int uep = UB + CV_USTR + 512 + toff * 2; \
    int hoff = (live ? WS_HT : WS_END) + d * 128 + (toff >> 6) * 81920 + (((toff & 63) * 2) ^ ((((d >> 1) & 1) | (((d >> 3) & 1) << 1)) << 5)); \
    int kw = KB + l16; \
    TIE(pa); TIE(pbb); TIE(xoff); TIE(uep); TIE(hoff); TIE(kw); TIE(uw);
#define CONV_FILLK(i, o) { \
    const unsigned d0 = kv[i][0], d1 = kv[i][1], d2 = kv[i][2], d3 = kv[i][3], d4 = kn[i][0], d5 = kn[i][1]; \
    const unsigned o0 = __builtin_amdgcn_alignbit(d1, d0, 16), o1 = __builtin_amdgcn_alignbit(d2, d1, 16), \
                   o2 = __builtin_amdgcn_alignbit(d3, d2, 16), o3 = __builtin_amdgcn_alignbit(d4, d3, 16), \
                   o4 = __builtin_amdgcn_alignbit(d5, d4, 16); \
    LP(v4u, kw + o + 3 * CV_KSTR) = v4u{d0, d1, d2, d3}; \
    LP(v4u, kw + o + 2 * CV_KSTR) = v4u{o0, o1, o2, o3}; \
    LP(v4u, kw + o + 1 * CV_KSTR) = v4u{d1, d2, d3, d4}; \
    LP(v4u, kw + o) = v4u{o1, o2, o3, o4}; }
#define CONV_FILLU(i, o) { \
    LP(v4u, uw + o + CV_USTR) = uv[i]; \
    LP(v2u, uw + o + 8) = v2u{uv[i][0], uv[i][1]}; \
    LP(v2u, uw + o + 16) = v2u{uv[i][2], uv[i][3]}; }
#define GRID_PUBLISH asm volatile("s_waitcnt vmcnt(0)" ::: "memory"); wg_barrier(); \
    if (threadIdx.x < GRP) __hip_atomic_fetch_add(ctr + GF_OFF + (threadIdx.x * GSH + gsh) * 32, 1u, RLX_AGENT);
#define LP(T, a) (*(LAS T*)(size_t)(unsigned)(a))
  const rsrc_t ro = __builtin_amdgcn_make_buffer_rsrc((void*)P.out, 0, L * D * 2, 0x00020000);

  v4u w1f[25], w2f[25], w3f[60], w4f[60], bf[5][4], af[16], bq[8];
  f32x4 cs1[5], cs3[3], own[5], ac1[3][5], sa[4], qa[4], acc[2][5], part[3], p15[15], base[5], a3[2][3], cs3a[3], gk3[4], stp, gt, gu;
  v2u hres[5], o5[5];
  float m1, r1;
  unsigned fv;
  GK gk;
  asm volatile("s_branch 1\nctramp:\n\ts_setpc_b64 s[22:23]");
  if (__builtin_expect(bx < CV_BLOCKS, 1)) {
  asm volatile("; MARK_BEGIN");
  CONV_ADDR_F
  LDV(kv[0], koff, 0, 0);
  LDV2(kn[0], koff, 0, 16);
  LDV(uv[0], uoff, WS_UT + 0, 0);
  LDV(kv[1], koff, 0, 1024);
  LDV2(kn[1], koff, 0, 1040);
  LDV(uv[1], uoff, WS_UT + 163840, 0);
  LDV(kv[2], koff, 0, 2048);
  LDV2(kn[2], koff, 0, 2064);
  LDV(uv[2], uoff, WS_UT + 327680, 0);
  LDV(kv[3], koff, 0, 3072);
  LDV2(kn[3], koff, 0, 3088);
  LDV(uv[3], uoff, WS_UT + 491520, 0);
  CONV_ZERO_F
  CONV_COMMON
  SB();
  asm volatile("s_mov_b64 s[24:25], exec\n\ts_mov_b64 exec, 0\n\ts_cmp_lg_u32 %0, 0\n\ts_cbranch_scc1 1\n\ts_call_b64 s[22:23], cpad_1\n\ts_cmp_lg_u32 %0, 1\n\ts_cbranch_scc1 1\n\ts_call_b64 s[22:23], cpad_2\n\ts_cmp_lg_u32 %0, 2\n\ts_cbranch_scc1 1\n\ts_call_b64 s[22:23], cpad_3\n\ts_cmp_lg_u32 %0, 3\n\ts_cbranch_scc1 1\n\ts_call_b64 s[22:23], cpad_4\n\ts_mov_b64 exec, s[24:25]" :: "s"(w) : "s22", "s23", "s24", "s25", "scc");
  asm volatile("s_mov_b64 s[24:25], exec\n\ts_mov_b64 exec, 0\n\ts_cmp_lg_u32 %0, 0\n\ts_cbranch_scc1 1\n\ts_call_b64 s[22:23], cpad_5\n\ts_cmp_lg_u32 %0, 1\n\ts_cbranch_scc1 1\n\ts_call_b64 s[22:23], cpad_6\n\ts_cmp_lg_u32 %0, 2\n\ts_cbranch_scc1 1\n\ts_call_b64 s[22:23], cpad_7\n\ts_cmp_lg_u32 %0, 3\n\ts_cbranch_scc1 1\n\ts_call_b64 s[22:23], ctramp\n\ts_mov_b64 exec, s[24:25]" :: "s"(w) : "s22", "s23", "s24", "s25", "scc"); SB();
  LDA(w1f[0], l16, w1b + 0, 0);
  LDA(w1f[1], l16, w1b + 0, 1024); SB();
  asm volatile("s_waitcnt vmcnt(12)" : "+v"(kv[0]), "+v"(kn[0]) :: "memory");
  CONV_FILLK(0, 0)
  LDA(w1f[2], l16, w1b + 0, 2048); SB();
  asm volatile("s_waitcnt vmcnt(12)" : "+v"(uv[0]) :: "memory");
  CONV_FILLU(0, 0)
  LDA(w1f[3], l16, w1b + 0, 3072); SB();
  asm volatile("s_waitcnt vmcnt(11)" : "+v"(kv[1]), "+v"(kn[1]) :: "memory");
  CONV_FILLK(1, 1024)
  LDA(w1f[4], l16, w1b + 4096, 0); SB();
  asm volatile("s_waitcnt vmcnt(11)" : "+v"(uv[1]) :: "memory");
  CONV_FILLU(1, 256)
  LDA(w1f[5], l16, w1b + 4096, 1024); SB();
  asm volatile("s_waitcnt vmcnt(10)" : "+v"(kv[2]), "+v"(kn[2]) :: "memory");
  CONV_FILLK(2, 2048)
  LDA(w1f[6], l16, w1b + 4096, 2048); SB();
  asm volatile("s_waitcnt vmcnt(10)" : "+v"(uv[2]) :: "memory");
  CONV_FILLU(2, 512)
  LDA(w1f[7], l16, w1b + 4096, 3072); SB();
  asm volatile("s_waitcnt vmcnt(9)" : "+v"(kv[3]), "+v"(kn[3]) :: "memory");
  CONV_FILLK(3, 3072)
  LDA(w1f[8], l16, w1b + 8192, 0); SB();
  asm volatile("s_waitcnt vmcnt(9)" : "+v"(uv[3]) :: "memory");
  CONV_FILLU(3, 768)
  LDV(w1f[9], l16, w1b + 8192, 1024); SB();
  wg_barrier();
  ta[0][0] = LP(v4u, pa + 4032);
  tb[0][4] = LP(v4u, pbb + 2496);
  tb[0][5] = LP(v4u, pbb + 3008);
  tb[0][6] = LP(v4u, pbb + 3520);
  tb[0][7] = LP(v4u, pbb + 4032);
  ta[0][1] = LP(v4u, pa + 3520);
  tb[0][3] = LP(v4u, pbb + 1984);
  ta[0][2] = LP(v4u, pa + 3008);
  tb[0][2] = LP(v4u, pbb + 1472);
  ta[0][3] = LP(v4u, pa + 2496);
  tb[0][1] = LP(v4u, pbb + 960);
  ta[0][4] = LP(v4u, pa + 1984);
  tb[0][0] = LP(v4u, pbb + 448);
  ta[0][5] = LP(v4u, pa + 1472);
  ta[0][6] = LP(v4u, pa + 960);
  ta[0][7] = LP(v4u, pa + 448); SB();
  ta[1][0] = LP(v4u, pa + 3968);
  cacc[4] = mfma(ta[0][0], tb[0][4], Z4); SB();
  dma_plain(rs, ldsb + CV_XL + w * 4096 + 0, l16, WS_XT + w * 4096 + 0);
  cacc[5] = mfma(ta[0][0], tb[0][5], Z4); SB();
  tb[1][4] = LP(v4u, pbb + 2432);
  cacc[6] = mfma(ta[0][0], tb[0][6], Z4); SB();
  tb[1][5] = LP(v4u, pbb + 2944);
  cacc[7] = mfma(ta[0][0], tb[0][7], Z4); SB();
  cacc[4] = mfma(ta[0][1], tb[0][3], cacc[4]); SB();
  tb[1][6] = LP(v4u, pbb + 3456);
  cacc[5] = mfma(ta[0][1], tb[0][4], cacc[5]); SB();
  tb[1][7] = LP(v4u, pbb + 3968);
  dma_plain(rs, ldsb + CV_XL + w * 4096 + 1024, l16, WS_XT + w * 4096 + 1024);
  cacc[6] = mfma(ta[0][1], tb[0][5], cacc[6]); SB();
  cacc[7] = mfma(ta[0][1], tb[0][6], cacc[7]); SB();
  ta[1][1] = LP(v4u, pa + 3456);
  cacc[4] = mfma(ta[0][2], tb[0][2], cacc[4]); SB();
  tb[1][3] = LP(v4u, pbb + 1920);
  cacc[5] = mfma(ta[0][2], tb[0][3], cacc[5]); SB();
  cacc[6] = mfma(ta[0][2], tb[0][4], cacc[6]); SB();
  ta[1][2] = LP(v4u, pa + 2944);
  dma_plain(rs, ldsb + CV_XL + w * 4096 + 2048, l16, WS_XT + w * 4096 + 2048);
  cacc[7] = mfma(ta[0][2], tb[0][5], cacc[7]); SB();
  tb[1][2] = LP(v4u, pbb + 1408);
  cacc[4] = mfma(ta[0][3], tb[0][1], cacc[4]); SB();
  ta[1][3] = LP(v4u, pa + 2432);
  cacc[5] = mfma(ta[0][3], tb[0][2], cacc[5]); SB();
  cacc[6] = mfma(ta[0][3], tb[0][3], cacc[6]); SB();
  tb[1][1] = LP(v4u, pbb + 896);
  cacc[7] = mfma(ta[0][3], tb[0][4], cacc[7]); SB();
  ta[1][4] = LP(v4u, pa + 1920);
  dma_plain(rs, ldsb + CV_XL + w * 4096 + 3072, l16, WS_XT + w * 4096 + 3072);
  cacc[4] = mfma(ta[0][4], tb[0][0], cacc[4]); SB();
  cacc[5] = mfma(ta[0][4], tb[0][1], cacc[5]); SB();
  tb[1][0] = LP(v4u, pbb + 384);
  cacc[6] = mfma(ta[0][4], tb[0][2], cacc[6]); SB();
  ta[1][5] = LP(v4u, pa + 1408);
  cacc[7] = mfma(ta[0][4], tb[0][3], cacc[7]); SB();
  cacc[5] = mfma(ta[0][5], tb[0][0], cacc[5]); SB();
  ta[1][6] = LP(v4u, pa + 896);
  LDV1(dsk[0], zero, dso, 0);
  cacc[6] = mfma(ta[0][5], tb[0][1], cacc[6]); SB();
  ta[1][7] = LP(v4u, pa + 384);
  cacc[7] = mfma(ta[0][5], tb[0][2], cacc[7]); SB();
  cacc[6] = mfma(ta[0][6], tb[0][0], cacc[6]); SB();
  cacc[7] = mfma(ta[0][6], tb[0][1], cacc[7]); SB();
  cacc[7] = mfma(ta[0][7], tb[0][0], cacc[7]); SB();
  ta[0][0] = LP(v4u, pa + 3904);
  LDV(w1f[10], l16, w1b + 8192, 2048);
  cacc[4] = mfma(ta[1][0], tb[1][4], cacc[4]); SB();
  cacc[5] = mfma(ta[1][0], tb[1][5], cacc[5]); SB();
  tb[0][4] = LP(v4u, pbb + 2368);
  cacc[6] = mfma(ta[1][0], tb[1][6], cacc[6]); SB();
  tb[0][5] = LP(v4u, pbb + 2880);
  cacc[7] = mfma(ta[1][0], tb[1][7], cacc[7]); SB();
  cacc[4] = mfma(ta[1][1], tb[1][3], cacc[4]); SB();
  tb[0][6] = LP(v4u, pbb + 3392);
  LDV(w1f[11], l16, w1b + 8192, 3072);
  cacc[5] = mfma(ta[1][1], tb[1][4], cacc[5]); SB();
  tb[0][7] = LP(v4u, pbb + 3904);
  cacc[6] = mfma(ta[1][1], tb[1][5], cacc[6]); SB();
  cacc[7] = mfma(ta[1][1], tb[1][6], cacc[7]); SB();
  ta[0][1] = LP(v4u, pa + 3392);
  cacc[4] = mfma(ta[1][2], tb[1][2], cacc[4]); SB();
  tb[0][3] = LP(v4u, pbb + 1856);
  cacc[5] = mfma(ta[1][2], tb[1][3], cacc[5]); SB();
  LDV(w1f[12], l16, w1b + 12288, 0);
  cacc[6] = mfma(ta[1][2], tb[1][4], cacc[6]); SB();
  ta[0][2] = LP(v4u, pa + 2880);
  cacc[7] = mfma(ta[1][2], tb[1][5], cacc[7]); SB();
  tb[0][2] = LP(v4u, pbb + 1344);
  cacc[4] = mfma(ta[1][3], tb[1][1], cacc[4]); SB();
  ta[0][3] = LP(v4u, pa + 2368);
  cacc[5] = mfma(ta[1][3], tb[1][2], cacc[5]); SB();
  cacc[6] = mfma(ta[1][3], tb[1][3], cacc[6]); SB();
  tb[0][1] = LP(v4u, pbb + 832);
  LDV(w1f[13], l16, w1b + 12288, 1024);
  cacc[7] = mfma(ta[1][3], tb[1][4], cacc[7]); SB();
  ta[0][4] = LP(v4u, pa + 1856);
  cacc[4] = mfma(ta[1][4], tb[1][0], cacc[4]); SB();
  cacc[5] = mfma(ta[1][4], tb[1][1], cacc[5]); SB();
  tb[0][0] = LP(v4u, pbb + 320);
  cacc[6] = mfma(ta[1][4], tb[1][2], cacc[6]); SB();
  ta[0][5] = LP(v4u, pa + 1344);
  cacc[7] = mfma(ta[1][4], tb[1][3], cacc[7]); SB();
  LDV(w1f[14], l16, w1b + 12288, 2048);
  cacc[5] = mfma(ta[1][5], tb[1][0], cacc[5]); SB();
  ta[0][6] = LP(v4u, pa + 832);
  cacc[6] = mfma(ta[1][5], tb[1][1], cacc[6]); SB();
  ta[0][7] = LP(v4u, pa + 320);
  cacc[7] = mfma(ta[1][5], tb[1][2], cacc[7]); SB();
  cacc[6] = mfma(ta[1][6], tb[1][0], cacc[6]); SB();
  cacc[7] = mfma(ta[1][6], tb[1][1], cacc[7]); SB();
  LDV(w1f[15], l16, w1b + 12288, 3072);
  cacc[7] = mfma(ta[1][7], tb[1][0], cacc[7]); SB();
  ta[1][0] = LP(v4u, pa + 3840);
  cacc[4] = mfma(ta[0][0], tb[0][4], cacc[4]); SB();
  cacc[5] = mfma(ta[0][0], tb[0][5], cacc[5]); SB();
  tb[1][4] = LP(v4u, pbb + 2304);
  cacc[6] = mfma(ta[0][0], tb[0][6], cacc[6]); SB();
  tb[1][5] = LP(v4u, pbb + 2816);
  cacc[7] = mfma(ta[0][0], tb[0][7], cacc[7]); SB();
  LDV(w1f[16], l16, w1b + 16384, 0);
  cacc[4] = mfma(ta[0][1], tb[0][3], cacc[4]); SB();
  tb[1][6] = LP(v4u, pbb + 3328);
  cacc[5] = mfma(ta[0][1], tb[0][4], cacc[5]); SB();
  tb[1][7] = LP(v4u, pbb + 3840);
  cacc[6] = mfma(ta[0][1], tb[0][5], cacc[6]); SB();
  cacc[7] = mfma(ta[0][1], tb[0][6], cacc[7]); SB();
  ta[1][1] = LP(v4u, pa + 3328);
  cacc[4] = mfma(ta[0][2], tb[0][2], cacc[4]); SB();
  tb[1][3] = LP(v4u, pbb + 1792);
  LDV(w1f[17], l16, w1b + 16384, 1024);
  cacc[5] = mfma(ta[0][2], tb[0][3], cacc[5]); SB();
  cacc[6] = mfma(ta[0][2], tb[0][4], cacc[6]); SB();
  ta[1][2] = LP(v4u, pa + 2816);
  cacc[7] = mfma(ta[0][2], tb[0][5], cacc[7]); SB();
  tb[1][2] = LP(v4u, pbb + 1280);
  cacc[4] = mfma(ta[0][3], tb[0][1], cacc[4]); SB();
  ta[1][3] = LP(v4u, pa + 2304);
  cacc[5] = mfma(ta[0][3], tb[0][2], cacc[5]); SB();
  LDV(w1f[18], l16, w1b + 16384, 2048);
  cacc[6] = mfma(ta[0][3], tb[0][3], cacc[6]); SB();
  tb[1][1] = LP(v4u, pbb + 768);
  cacc[7] = mfma(ta[0][3], tb[0][4], cacc[7]); SB();
  ta[1][4] = LP(v4u, pa + 1792);
  cacc[4] = mfma(ta[0][4], tb[0][0], cacc[4]); SB();
  cacc[5] = mfma(ta[0][4], tb[0][1], cacc[5]); SB();
  tb[1][0] = LP(v4u, pbb + 256);
  cacc[6] = mfma(ta[0][4], tb[0][2], cacc[6]); SB();
  ta[1][5] = LP(v4u, pa + 1280);
  LDV(w1f[19], l16, w1b + 16384, 3072);
  cacc[7] = mfma(ta[0][4], tb[0][3], cacc[7]); SB();
  cacc[5] = mfma(ta[0][5], tb[0][0], cacc[5]); SB();
  ta[1][6] = LP(v4u, pa + 768);
  cacc[6] = mfma(ta[0][5], tb[0][1], cacc[6]); SB();
  ta[1][7] = LP(v4u, pa + 256);
  cacc[7] = mfma(ta[0][5], tb[0][2], cacc[7]); SB();
  cacc[6] = mfma(ta[0][6], tb[0][0], cacc[6]); SB();
  LDV(w1f[20], l16, w1b + 20480, 0);
  cacc[7] = mfma(ta[0][6], tb[0][1], cacc[7]); SB();
  cacc[7] = mfma(ta[0][7], tb[0][0], cacc[7]); SB();
  ta[0][0] = LP(v4u, pa + 3776);
  cacc[4] = mfma(ta[1][0], tb[1][4], cacc[4]); SB();
  cacc[5] = mfma(ta[1][0], tb[1][5], cacc[5]); SB();
  tb[0][4] = LP(v4u, pbb + 2240);
  cacc[6] = mfma(ta[1][0], tb[1][6], cacc[6]); SB();
  tb[0][5] = LP(v4u, pbb + 2752);
  LDV(w1f[21], l16, w1b + 20480, 1024);
  cacc[7] = mfma(ta[1][0], tb[1][7], cacc[7]); SB();
  cacc[4] = mfma(ta[1][1], tb[1][3], cacc[4]); SB();
  tb[0][6] = LP(v4u, pbb + 3264);
  cacc[5] = mfma(ta[1][1], tb[1][4], cacc[5]); SB();
  tb[0][7] = LP(v4u, pbb + 3776);
  cacc[6] = mfma(ta[1][1], tb[1][5], cacc[6]); SB();
  cacc[7] = mfma(ta[1][1], tb[1][6], cacc[7]); SB();
  ta[0][1] = LP(v4u, pa + 3264);
  LDV(w1f[22], l16, w1b + 20480, 2048);
  cacc[4] = mfma(ta[1][2], tb[1][2], cacc[4]); SB();
  tb[0][3] = LP(v4u, pbb + 1728);
  cacc[5] = mfma(ta[1][2], tb[1][3], cacc[5]); SB();
  cacc[6] = mfma(ta[1][2], tb[1][4], cacc[6]); SB();
  ta[0][2] = LP(v4u, pa + 2752);
  cacc[7] = mfma(ta[1][2], tb[1][5], cacc[7]); SB();
  tb[0][2] = LP(v4u, pbb + 1216);
  cacc[4] = mfma(ta[1][3], tb[1][1], cacc[4]); SB();
  ta[0][3] = LP(v4u, pa + 2240);
  LDV(w1f[23], l16, w1b + 20480, 3072);
  cacc[5] = mfma(ta[1][3], tb[1][2], cacc[5]); SB();
  cacc[6] = mfma(ta[1][3], tb[1][3], cacc[6]); SB();
  tb[0][1] = LP(v4u, pbb + 704);
  cacc[7] = mfma(ta[1][3], tb[1][4], cacc[7]); SB();
  ta[0][4] = LP(v4u, pa + 1728);
  cacc[4] = mfma(ta[1][4], tb[1][0], cacc[4]); SB();
  cacc[5] = mfma(ta[1][4], tb[1][1], cacc[5]); SB();
  tb[0][0] = LP(v4u, pbb + 192);
  LDV(w1f[24], l16, w1b + 24576, 0);
  cacc[6] = mfma(ta[1][4], tb[1][2], cacc[6]); SB();
  ta[0][5] = LP(v4u, pa + 1216);
  cacc[7] = mfma(ta[1][4], tb[1][3], cacc[7]); SB();
  cacc[5] = mfma(ta[1][5], tb[1][0], cacc[5]); SB();
  ta[0][6] = LP(v4u, pa + 704);
  cacc[6] = mfma(ta[1][5], tb[1][1], cacc[6]); SB();
  ta[0][7] = LP(v4u, pa + 192);
  cacc[7] = mfma(ta[1][5], tb[1][2], cacc[7]); SB();
  LDA(w2f[0], l16, w2b + 0, 0);
  cacc[6] = mfma(ta[1][6], tb[1][0], cacc[6]); SB();
  cacc[7] = mfma(ta[1][6], tb[1][1], cacc[7]); SB();
  cacc[7] = mfma(ta[1][7], tb[1][0], cacc[7]); SB();
  asm volatile("s_waitcnt vmcnt(16)" : "+v"(dsk[0]) :: "memory");
  wg_barrier(); SB();
  ta[1][0] = LP(v4u, pa + 3712);
  cacc[4] = mfma(ta[0][0], tb[0][4], cacc[4]); SB();
  uu = LP(v2u, uep + 2048); gu = LP(f32x4, xoff + 8192); SB();
  cacc[5] = mfma(ta[0][0], tb[0][5], cacc[5]); SB();
  cb[4] = LP(f32x4, xoff + 8256); SB();
  tb[1][4] = LP(v4u, pbb + 2176);
  LDA(w2f[1], l16, w2b + 0, 1024);
  cacc[6] = mfma(ta[0][0], tb[0][6], cacc[6]); SB();
  tb[1][5] = LP(v4u, pbb + 2688);
  cacc[7] = mfma(ta[0][0], tb[0][7], cacc[7]); SB();
  cacc[4] = mfma(ta[0][1], tb[0][3], cacc[4]); SB();
  tb[1][6] = LP(v4u, pbb + 3200);
  cacc[5] = mfma(ta[0][1], tb[0][4], cacc[5]); SB();
  UNP2(gt[0], gt[1], uu[0]); SB();
  tb[1][7] = LP(v4u, pbb + 3712);
  cacc[6] = mfma(ta[0][1], tb[0][5], cacc[6]); SB();
  UNP2(gt[2], gt[3], uu[1]); SB();
  LDA(w2f[2], l16, w2b + 0, 2048);
  cacc[7] = mfma(ta[0][1], tb[0][6], cacc[7]); SB();
  ADDK2(gu[0], gu[1], dsk[0]); SB();
  ta[1][1] = LP(v4u, pa + 3200);
  cacc[4] = mfma(ta[0][2], tb[0][2], cacc[4]); SB();
  ADDK2(gu[2], gu[3], dsk[0]); SB();
  tb[1][3] = LP(v4u, pbb + 1664);
  cacc[5] = mfma(ta[0][2], tb[0][3], cacc[5]); SB();
  FMAC2B(cb[4][0], cb[4][1], gt[0], gt[1], gu[0], gu[1]); SB();
  cacc[6] = mfma(ta[0][2], tb[0][4], cacc[6]); SB();
  FMAC2B(cb[4][2], cb[4][3], gt[2], gt[3], gu[2], gu[3]); SB();
  ta[1][2] = LP(v4u, pa + 2688);
  cacc[7] = mfma(ta[0][2], tb[0][5], cacc[7]); SB();
  uu = LP(v2u, uep + 2560); gu = LP(f32x4, xoff + 10240); SB();
  tb[1][2] = LP(v4u, pbb + 1152);
  LDA(w2f[3], l16, w2b + 0, 3072);
  cacc[4] = mfma(ta[0][3], tb[0][1], cacc[4]); SB();
  cb[5] = LP(f32x4, xoff + 10304); SB();
  ta[1][3] = LP(v4u, pa + 2176);
  cacc[5] = mfma(ta[0][3], tb[0][2], cacc[5]); SB();
  cacc[6] = mfma(ta[0][3], tb[0][3], cacc[6]); SB();
  tb[1][1] = LP(v4u, pbb + 640);
  cacc[7] = mfma(ta[0][3], tb[0][4], cacc[7]); SB();
  ta[1][4] = LP(v4u, pa + 1664);
  cacc[4] = mfma(ta[0][4], tb[0][0], cacc[4]); SB();
  UNP2(gt[0], gt[1], uu[0]); SB();
  LDA(w2f[4], l16, w2b + 4096, 0);
  cacc[5] = mfma(ta[0][4], tb[0][1], cacc[5]); SB();
  UNP2(gt[2], gt[3], uu[1]); SB();
  tb[1][0] = LP(v4u, pbb + 128);
  cacc[6] = mfma(ta[0][4], tb[0][2], cacc[6]); SB();
  ADDK2(gu[0], gu[1], dsk[0]); SB();
  ta[1][5] = LP(v4u, pa + 1152);
  cacc[7] = mfma(ta[0][4], tb[0][3], cacc[7]); SB();
  ADDK2(gu[2], gu[3], dsk[0]); SB();
  PAD(1); SB();
  cacc[5] = mfma(ta[0][5], tb[0][0], cacc[5]); SB();
  FMAC2B(cb[5][0], cb[5][1], gt[0], gt[1], gu[0], gu[1]); SB();
  ta[1][6] = LP(v4u, pa + 640);
  cacc[6] = mfma(ta[0][5], tb[0][1], cacc[6]); SB();
  FMAC2B(cb[5][2], cb[5][3], gt[2], gt[3], gu[2], gu[3]); SB();
  ta[1][7] = LP(v4u, pa + 128);
  LDA(w2f[5], l16, w2b + 4096, 1024);
  cacc[7] = mfma(ta[0][5], tb[0][2], cacc[7]); SB();
  uu = LP(v2u, uep + 3072); gu = LP(f32x4, xoff + 12288); SB();
  cacc[6] = mfma(ta[0][6], tb[0][0], cacc[6]); SB();
  cb[6] = LP(f32x4, xoff + 12352); SB();
  cacc[7] = mfma(ta[0][6], tb[0][1], cacc[7]); SB();
  cacc[7] = mfma(ta[0][7], tb[0][0], cacc[7]); SB();
  ta[0][0] = LP(v4u, pa + 3648);
  cacc[4] = mfma(ta[1][0], tb[1][4], cacc[4]); SB();
  LDA(w2f[6], l16, w2b + 4096, 2048);
  cacc[5] = mfma(ta[1][0], tb[1][5], cacc[5]); SB();
  UNP2(gt[0], gt[1], uu[0]); SB();
  tb[0][4] = LP(v4u, pbb + 2112);
  cacc[6] = mfma(ta[1][0], tb[1][6], cacc[6]); SB();
  UNP2(gt[2], gt[3], uu[1]); SB();
  tb[0][5] = LP(v4u, pbb + 2624);
  cacc[7] = mfma(ta[1][0], tb[1][7], cacc[7]); SB();
  ADDK2(gu[0], gu[1], dsk[0]); SB();
  cacc[4] = mfma(ta[1][1], tb[1][3], cacc[4]); SB();
  ADDK2(gu[2], gu[3], dsk[0]); SB();
  tb[0][6] = LP(v4u, pbb + 3136);
  cacc[5] = mfma(ta[1][1], tb[1][4], cacc[5]); SB();
  FMAC2B(cb[6][0], cb[6][1], gt[0], gt[1], gu[0], gu[1]); SB();
  tb[0][7] = LP(v4u, pbb + 3648);
  LDA(w2f[7], l16, w2b + 4096, 3072);
  cacc[6] = mfma(ta[1][1], tb[1][5], cacc[6]); SB();
  FMAC2B(cb[6][2], cb[6][3], gt[2], gt[3], gu[2], gu[3]); SB();
  cacc[7] = mfma(ta[1][1], tb[1][6], cacc[7]); SB();
  uu = LP(v2u, uep + 3584); gu = LP(f32x4, xoff + 14336); SB();
  ta[0][1] = LP(v4u, pa + 3136);
  cacc[4] = mfma(ta[1][2], tb[1][2], cacc[4]); SB();
  cb[7] = LP(f32x4, xoff + 14400); SB();
  tb[0][3] = LP(v4u, pbb + 1600);
  cacc[5] = mfma(ta[1][2], tb[1][3], cacc[5]); SB();
  cacc[6] = mfma(ta[1][2], tb[1][4], cacc[6]); SB();
  ta[0][2] = LP(v4u, pa + 2624);
  LDA(w2f[8], l16, w2b + 8192, 0);
  cacc[7] = mfma(ta[1][2], tb[1][5], cacc[7]); SB();
  tb[0][2] = LP(v4u, pbb + 1088);
  cacc[4] = mfma(ta[1][3], tb[1][1], cacc[4]); SB();
  UNP2(gt[0], gt[1], uu[0]); SB();
  ta[0][3] = LP(v4u, pa + 2112);
  cacc[5] = mfma(ta[1][3], tb[1][2], cacc[5]); SB();
  UNP2(gt[2], gt[3], uu[1]); SB();
  cacc[6] = mfma(ta[1][3], tb[1][3], cacc[6]); SB();
  ADDK2(gu[0], gu[1], dsk[0]); SB();
  tb[0][1] = LP(v4u, pbb + 576);
  cacc[7] = mfma(ta[1][3], tb[1][4], cacc[7]); SB();
  ADDK2(gu[2], gu[3], dsk[0]); SB();
  ta[0][4] = LP(v4u, pa + 1600);
  LDA(w2f[9], l16, w2b + 8192, 1024);
  cacc[4] = mfma(ta[1][4], tb[1][0], cacc[4]); SB();
  FMAC2B(cb[7][0], cb[7][1], gt[0], gt[1], gu[0], gu[1]); SB();
  cacc[5] = mfma(ta[1][4], tb[1][1], cacc[5]); SB();
  FMAC2B(cb[7][2], cb[7][3], gt[2], gt[3], gu[2], gu[3]); SB();
  tb[0][0] = LP(v4u, pbb + 64);
  cacc[6] = mfma(ta[1][4], tb[1][2], cacc[6]); SB();
  ta[0][5] = LP(v4u, pa + 1088);
  cacc[7] = mfma(ta[1][4], tb[1][3], cacc[7]); SB();
  cacc[5] = mfma(ta[1][5], tb[1][0], cacc[5]); SB();
  ta[0][6] = LP(v4u, pa + 576);
  LDA(w2f[10], l16, w2b + 8192, 2048);
  cacc[6] = mfma(ta[1][5], tb[1][1], cacc[6]); SB();
  ta[0][7] = LP(v4u, pa + 64);
  cacc[7] = mfma(ta[1][5], tb[1][2], cacc[7]); SB();
  cacc[6] = mfma(ta[1][6], tb[1][0], cacc[6]); SB();
  cacc[7] = mfma(ta[1][6], tb[1][1], cacc[7]); SB();
  cacc[7] = mfma(ta[1][7], tb[1][0], cacc[7]); SB();
  ta[1][0] = LP(v4u, pa + 3584);
  LDA(w2f[11], l16, w2b + 8192, 3072);
  cacc[4] = mfma(ta[0][0], tb[0][4], cacc[4]); SB();
  cacc[5] = mfma(ta[0][0], tb[0][5], cacc[5]); SB();
  tb[1][4] = LP(v4u, pbb + 2048);
  cacc[6] = mfma(ta[0][0], tb[0][6], cacc[6]); SB();
  tb[1][5] = LP(v4u, pbb + 2560);
  cacc[7] = mfma(ta[0][0], tb[0][7], cacc[7]); SB();
  cacc[4] = mfma(ta[0][1], tb[0][3], cacc[4]); SB();
  tb[1][6] = LP(v4u, pbb + 3072);
  LDA(w2f[12], l16, w2b + 12288, 0);
  cacc[5] = mfma(ta[0][1], tb[0][4], cacc[5]); SB();
  tb[1][7] = LP(v4u, pbb + 3584);
  cacc[6] = mfma(ta[0][1], tb[0][5], cacc[6]); SB();
  cacc[7] = mfma(ta[0][1], tb[0][6], cacc[7]); SB();
  ta[1][1] = LP(v4u, pa + 3072);
  cacc[4] = mfma(ta[0][2], tb[0][2], cacc[4]); SB();
  tb[1][3] = LP(v4u, pbb + 1536);
  cacc[5] = mfma(ta[0][2], tb[0][3], cacc[5]); SB();
  LDA(w2f[13], l16, w2b + 12288, 1024);
  cacc[6] = mfma(ta[0][2], tb[0][4], cacc[6]); SB();
  ta[1][2] = LP(v4u, pa + 2560);
  cacc[7] = mfma(ta[0][2], tb[0][5], cacc[7]); SB();
  tb[1][2] = LP(v4u, pbb + 1024);
  cacc[4] = mfma(ta[0][3], tb[0][1], cacc[4]); SB();
  ta[1][3] = LP(v4u, pa + 2048);
  cacc[5] = mfma(ta[0][3], tb[0][2], cacc[5]); SB();
  cacc[6] = mfma(ta[0][3], tb[0][3], cacc[6]); SB();
  tb[1][1] = LP(v4u, pbb + 512);
  LDA(w2f[14], l16, w2b + 12288, 2048);
  cacc[7] = mfma(ta[0][3], tb[0][4], cacc[7]); SB();
  ta[1][4] = LP(v4u, pa + 1536);
  cacc[4] = mfma(ta[0][4], tb[0][0], cacc[4]); SB();
  cacc[5] = mfma(ta[0][4], tb[0][1], cacc[5]); SB();
  tb[1][0] = LP(v4u, pbb + 0);
  cacc[6] = mfma(ta[0][4], tb[0][2], cacc[6]); SB();
  ta[1][5] = LP(v4u, pa + 1024);
  cacc[7] = mfma(ta[0][4], tb[0][3], cacc[7]); SB();
  LDA(w2f[15], l16, w2b + 12288, 3072);
  cacc[5] = mfma(ta[0][5], tb[0][0], cacc[5]); SB();
  ta[1][6] = LP(v4u, pa + 512);
  cacc[6] = mfma(ta[0][5], tb[0][1], cacc[6]); SB();
  ta[1][7] = LP(v4u, pa + 0);
  cacc[7] = mfma(ta[0][5], tb[0][2], cacc[7]); SB();
  cacc[6] = mfma(ta[0][6], tb[0][0], cacc[6]); SB();
  cacc[7] = mfma(ta[0][6], tb[0][1], cacc[7]); SB();
  LDA(w2f[16], l16, w2b + 16384, 0);
  cacc[7] = mfma(ta[0][7], tb[0][0], cacc[7]); SB();
  cacc[7] = mfma(ta[1][0], tb[1][7], cacc[7]); SB();
  cacc[7] = mfma(ta[1][1], tb[1][6], cacc[7]); SB();
  cacc[7] = mfma(ta[1][2], tb[1][5], cacc[7]); SB();
  cacc[7] = mfma(ta[1][3], tb[1][4], cacc[7]); SB();
  cacc[7] = mfma(ta[1][4], tb[1][3], cacc[7]); SB();
  cacc[7] = mfma(ta[1][5], tb[1][2], cacc[7]); SB();
  cacc[7] = mfma(ta[1][6], tb[1][1], cacc[7]); SB();
  cacc[7] = mfma(ta[1][7], tb[1][0], cacc[7]); SB();
  cacc[6] = mfma(ta[1][0], tb[1][6], cacc[6]); SB();
  cacc[6] = mfma(ta[1][1], tb[1][5], cacc[6]); SB();
  cacc[6] = mfma(ta[1][2], tb[1][4], cacc[6]); SB();
  cacc[6] = mfma(ta[1][3], tb[1][3], cacc[6]); SB();
  cacc[6] = mfma(ta[1][4], tb[1][2], cacc[6]); SB();
  cacc[6] = mfma(ta[1][5], tb[1][1], cacc[6]); SB();
  gt = cacc[7]; TIE(gt); SB();
  cacc[6] = mfma(ta[1][6], tb[1][0], cacc[6]); SB();
  ADD2(cb[7][0], cb[7][1], gt[0], gt[1]); SB();
  cacc[5] = mfma(ta[1][0], tb[1][5], cacc[5]); SB();
  ADD2(cb[7][2], cb[7][3], gt[2], gt[3]); SB();
  cacc[5] = mfma(ta[1][1], tb[1][4], cacc[5]); SB();
  CVT2(uu[0], uu[1], cb[7]); SB();
  cacc[5] = mfma(ta[1][2], tb[1][3], cacc[5]); SB();
  ST2(uu, hoff + 2293760); SB();
  cacc[5] = mfma(ta[1][3], tb[1][2], cacc[5]); SB();
  cacc[5] = mfma(ta[1][4], tb[1][1], cacc[5]); SB();
  cacc[5] = mfma(ta[1][5], tb[1][0], cacc[5]); SB();
  gt = cacc[6]; TIE(gt); SB();
  cacc[4] = mfma(ta[1][0], tb[1][4], cacc[4]); SB();
  ADD2(cb[6][0], cb[6][1], gt[0], gt[1]); SB();
  cacc[4] = mfma(ta[1][1], tb[1][3], cacc[4]); SB();
  ADD2(cb[6][2], cb[6][3], gt[2], gt[3]); SB();
  cacc[4] = mfma(ta[1][2], tb[1][2], cacc[4]); SB();
  CVT2(uu[0], uu[1], cb[6]); SB();
  cacc[4] = mfma(ta[1][3], tb[1][1], cacc[4]); SB();
  ST2(uu, hoff + 1966080); SB();
  cacc[4] = mfma(ta[1][4], tb[1][0], cacc[4]); SB();
  gt = cacc[5]; TIE(gt); SB();
  ADD2(cb[5][0], cb[5][1], gt[0], gt[1]); SB();
  ADD2(cb[5][2], cb[5][3], gt[2], gt[3]); SB();
  CVT2(uu[0], uu[1], cb[5]); SB();
  ST2(uu, hoff + 1638400); SB();
  cb[4] += cacc[4]; ST2((v2u{pk2(cb[4][0], cb[4][1]), pk2(cb[4][2], cb[4][3])}), hoff + 1310720); SB();
  ADDR_B
  SB();
  GRID_PUBLISH
  asm volatile("; MARK_END");
  } else {
  asm volatile("; MARK_BEGIN");
  CONV_ADDR_E
  LDV(kv[0], koff, 0, 2048);
  LDV2(kn[0], koff, 0, 2064);
  LDV(uv[0], uoff, WS_UT + 0, 0);
  LDV(kv[1], koff, 0, 3072);
  LDV2(kn[1], koff, 0, 3088);
  LDV(uv[1], uoff, WS_UT + 81920, 0);
  LDV(kv[2], koff, 4224, 2048);
  LDV2(kn[2], koff, 4224, 2064);
  LDV(uv[2], uoff, WS_UT + 163840, 0);
  LDV(kv[3], koff, 4224, 3072);
  LDV2(kn[3], koff, 4224, 3088);
  LDV(uv[3], uoff, WS_UT + 245760, 0);
  CONV_ZERO_E
  CONV_COMMON
  SB();
  asm volatile("s_mov_b64 s[24:25], exec\n\ts_mov_b64 exec, 0\n\ts_cmp_lg_u32 %0, 0\n\ts_cbranch_scc1 1\n\ts_call_b64 s[22:23], cpad_8\n\ts_cmp_lg_u32 %0, 1\n\ts_cbranch_scc1 1\n\ts_call_b64 s[22:23], cpad_2\n\ts_cmp_lg_u32 %0, 2\n\ts_cbranch_scc1 1\n\ts_call_b64 s[22:23], cpad_3\n\ts_cmp_lg_u32 %0, 3\n\ts_cbranch_scc1 1\n\ts_call_b64 s[22:23], cpad_4\n\ts_mov_b64 exec, s[24:25]" :: "s"(w) : "s22", "s23", "s24", "s25", "scc");
  asm volatile("s_mov_b64 s[24:25], exec\n\ts_mov_b64 exec, 0\n\ts_cmp_lg_u32 %0, 0\n\ts_cbranch_scc1 1\n\ts_call_b64 s[22:23], cpad_5\n\ts_cmp_lg_u32 %0, 1\n\ts_cbranch_scc1 1\n\ts_call_b64 s[22:23], cpad_6\n\ts_cmp_lg_u32 %0, 2\n\ts_cbranch_scc1 1\n\ts_call_b64 s[22:23], cpad_7\n\ts_cmp_lg_u32 %0, 3\n\ts_cbranch_scc1 1\n\ts_call_b64 s[22:23], ctramp\n\ts_mov_b64 exec, s[24:25]" :: "s"(w) : "s22", "s23", "s24", "s25", "scc"); SB();
  LDA(w1f[0], l16, w1b + 0, 0);
  LDA(w1f[1], l16, w1b + 0, 1024); SB();
  asm volatile("s_waitcnt vmcnt(12)" : "+v"(kv[0]), "+v"(kn[0]) :: "memory");
  CONV_FILLK(0, 0)
  LDA(w1f[2], l16, w1b + 0, 2048); SB();
  asm volatile("s_waitcnt vmcnt(12)" : "+v"(uv[0]) :: "memory");
  CONV_FILLU(0, 0)
  LDA(w1f[3], l16, w1b + 0, 3072); SB();
  asm volatile("s_waitcnt vmcnt(11)" : "+v"(kv[1]), "+v"(kn[1]) :: "memory");
  CONV_FILLK(1, 1024)
  LDA(w1f[4], l16, w1b + 4096, 0); SB();
  asm volatile("s_waitcnt vmcnt(11)" : "+v"(uv[1]) :: "memory");
  CONV_FILLU(1, 128)
  LDA(w1f[5], l16, w1b + 4096, 1024); SB();
  asm volatile("s_waitcnt vmcnt(10)" : "+v"(kv[2]), "+v"(kn[2]) :: "memory");
  CONV_FILLK(2, 2112)
  LDA(w1f[6], l16, w1b + 4096, 2048); SB();
  asm volatile("s_waitcnt vmcnt(10)" : "+v"(uv[2]) :: "memory");
  CONV_FILLU(2, 256)
  LDA(w1f[7], l16, w1b + 4096, 3072); SB();
  asm volatile("s_waitcnt vmcnt(9)" : "+v"(kv[3]), "+v"(kn[3]) :: "memory");
  CONV_FILLK(3, 3136)
  LDA(w1f[8], l16, w1b + 8192, 0); SB();
  asm volatile("s_waitcnt vmcnt(9)" : "+v"(uv[3]) :: "memory");
  CONV_FILLU(3, 384)
  LDV(w1f[9], l16, w1b + 8192, 1024); SB();
  wg_barrier();
  ta[0][0] = LP(v4u, pa + 1984);
  tb[0][0] = LP(v4u, pbb + 448);
  tb[0][1] = LP(v4u, pbb + 960);
  tb[0][2] = LP(v4u, pbb + 1472);
  tb[0][3] = LP(v4u, pbb + 1984);
  ta[0][1] = LP(v4u, pa + 1472);
  ta[0][2] = LP(v4u, pa + 960);
  ta[0][3] = LP(v4u, pa + 448);
  ta[0][4] = LP(v4u, pa + 4096);
  tb[0][4] = LP(v4u, pbb + 3024);
  tb[0][5] = LP(v4u, pbb + 3536);
  tb[0][6] = LP(v4u, pbb + 4048);
  tb[0][7] = LP(v4u, pbb + 4560);
  ta[0][5] = LP(v4u, pa + 3584);
  ta[0][6] = LP(v4u, pa + 3072);
  ta[0][7] = LP(v4u, pa + 2560); SB();
  ta[1][0] = LP(v4u, pa + 1920);
  cacc[0] = mfma(ta[0][0], tb[0][0], Z4); SB();
  dma_plain(rs, ldsb + CV_XL + w * 4096 + 0, l16, WS_XT + w * 4096 + 0);
  cacc[1] = mfma(ta[0][0], tb[0][1], Z4); SB();
  tb[1][0] = LP(v4u, pbb + 384);
  cacc[2] = mfma(ta[0][0], tb[0][2], Z4); SB();
  tb[1][1] = LP(v4u, pbb + 896);
  cacc[3] = mfma(ta[0][0], tb[0][3], Z4); SB();
  tb[1][2] = LP(v4u, pbb + 1408);
  cacc[1] = mfma(ta[0][1], tb[0][0], cacc[1]); SB();
  tb[1][3] = LP(v4u, pbb + 1920);
  dma_plain(rs, ldsb + CV_XL + w * 4096 + 1024, l16, WS_XT + w * 4096 + 1024);
  cacc[2] = mfma(ta[0][1], tb[0][1], cacc[2]); SB();
  ta[1][1] = LP(v4u, pa + 1408);
  cacc[3] = mfma(ta[0][1], tb[0][2], cacc[3]); SB();
  ta[1][2] = LP(v4u, pa + 896);
  cacc[2] = mfma(ta[0][2], tb[0][0], cacc[2]); SB();
  ta[1][3] = LP(v4u, pa + 384);
  cacc[3] = mfma(ta[0][2], tb[0][1], cacc[3]); SB();
  ta[1][4] = LP(v4u, pa + 4032);
  dma_plain(rs, ldsb + CV_XL + w * 4096 + 2048, l16, WS_XT + w * 4096 + 2048);
  cacc[3] = mfma(ta[0][3], tb[0][0], cacc[3]); SB();
  tb[1][4] = LP(v4u, pbb + 2960);
  cacc[4] = mfma(ta[0][4], tb[0][4], Z4); SB();
  tb[1][5] = LP(v4u, pbb + 3472);
  cacc[5] = mfma(ta[0][4], tb[0][5], Z4); SB();
  tb[1][6] = LP(v4u, pbb + 3984);
  cacc[6] = mfma(ta[0][4], tb[0][6], Z4); SB();
  tb[1][7] = LP(v4u, pbb + 4496);
  dma_plain(rs, ldsb + CV_XL + w * 4096 + 3072, l16, WS_XT + w * 4096 + 3072);
  cacc[7] = mfma(ta[0][4], tb[0][7], Z4); SB();
  ta[1][5] = LP(v4u, pa + 3520);
  cacc[5] = mfma(ta[0][5], tb[0][4], cacc[5]); SB();
  ta[1][6] = LP(v4u, pa + 3008);
  cacc[6] = mfma(ta[0][5], tb[0][5], cacc[6]); SB();
  ta[1][7] = LP(v4u, pa + 2496);
  cacc[7] = mfma(ta[0][5], tb[0][6], cacc[7]); SB();
  LDV1(dsk[1], zero, dso + 4224, 0);
  LDV1(dsk[0], zero, dso, 0);
  cacc[6] = mfma(ta[0][6], tb[0][4], cacc[6]); SB();
  cacc[7] = mfma(ta[0][6], tb[0][5], cacc[7]); SB();
  cacc[7] = mfma(ta[0][7], tb[0][4], cacc[7]); SB();
  ta[0][0] = LP(v4u, pa + 1856);
  cacc[0] = mfma(ta[1][0], tb[1][0], cacc[0]); SB();
  LDV(w1f[10], l16, w1b + 8192, 2048);
  cacc[1] = mfma(ta[1][0], tb[1][1], cacc[1]); SB();
  tb[0][0] = LP(v4u, pbb + 320);
  cacc[2] = mfma(ta[1][0], tb[1][2], cacc[2]); SB();
  tb[0][1] = LP(v4u, pbb + 832);
  cacc[3] = mfma(ta[1][0], tb[1][3], cacc[3]); SB();
  tb[0][2] = LP(v4u, pbb + 1344);
  cacc[1] = mfma(ta[1][1], tb[1][0], cacc[1]); SB();
  tb[0][3] = LP(v4u, pbb + 1856);
  LDV(w1f[11], l16, w1b + 8192, 3072);
  cacc[2] = mfma(ta[1][1], tb[1][1], cacc[2]); SB();
  ta[0][1] = LP(v4u, pa + 1344);
  cacc[3] = mfma(ta[1][1], tb[1][2], cacc[3]); SB();
  ta[0][2] = LP(v4u, pa + 832);
  cacc[2] = mfma(ta[1][2], tb[1][0], cacc[2]); SB();
  ta[0][3] = LP(v4u, pa + 320);
  cacc[3] = mfma(ta[1][2], tb[1][1], cacc[3]); SB();
  ta[0][4] = LP(v4u, pa + 3968);
  LDV(w1f[12], l16, w1b + 12288, 0);
  cacc[3] = mfma(ta[1][3], tb[1][0], cacc[3]); SB();
  tb[0][4] = LP(v4u, pbb + 2896);
  cacc[4] = mfma(ta[1][4], tb[1][4], cacc[4]); SB();
  tb[0][5] = LP(v4u, pbb + 3408);
  cacc[5] = mfma(ta[1][4], tb[1][5], cacc[5]); SB();
  tb[0][6] = LP(v4u, pbb + 3920);
  cacc[6] = mfma(ta[1][4], tb[1][6], cacc[6]); SB();
  tb[0][7] = LP(v4u, pbb + 4432);
  LDV(w1f[13], l16, w1b + 12288, 1024);
  cacc[7] = mfma(ta[1][4], tb[1][7], cacc[7]); SB();
  ta[0][5] = LP(v4u, pa + 3456);
  cacc[5] = mfma(ta[1][5], tb[1][4], cacc[5]); SB();
  ta[0][6] = LP(v4u, pa + 2944);
  cacc[6] = mfma(ta[1][5], tb[1][5], cacc[6]); SB();
  ta[0][7] = LP(v4u, pa + 2432);
  cacc[7] = mfma(ta[1][5], tb[1][6], cacc[7]); SB();
  LDV(w1f[14], l16, w1b + 12288, 2048);
  cacc[6] = mfma(ta[1][6], tb[1][4], cacc[6]); SB();
  cacc[7] = mfma(ta[1][6], tb[1][5], cacc[7]); SB();
  cacc[7] = mfma(ta[1][7], tb[1][4], cacc[7]); SB();
  asm volatile("s_waitcnt vmcnt(5)" : "+v"(dsk[0]), "+v"(dsk[1]) :: "memory");
  wg_barrier(); SB();
  ta[1][0] = LP(v4u, pa + 1792);
  cacc[0] = mfma(ta[0][0], tb[0][0], cacc[0]); SB();
  uu = LP(v2u, uep + 0); gu = LP(f32x4, xoff + 0); SB();
  LDV(w1f[15], l16, w1b + 12288, 3072);
  cacc[1] = mfma(ta[0][0], tb[0][1], cacc[1]); SB();
  cb[0] = LP(f32x4, xoff + 64); SB();
  tb[1][0] = LP(v4u, pbb + 256);
  cacc[2] = mfma(ta[0][0], tb[0][2], cacc[2]); SB();
  tb[1][1] = LP(v4u, pbb + 768);
  cacc[3] = mfma(ta[0][0], tb[0][3], cacc[3]); SB();
  tb[1][2] = LP(v4u, pbb + 1280);
  cacc[1] = mfma(ta[0][1], tb[0][0], cacc[1]); SB();
  tb[1][3] = LP(v4u, pbb + 1792);
  LDV(w1f[16], l16, w1b + 16384, 0);
  cacc[2] = mfma(ta[0][1], tb[0][1], cacc[2]); SB();
  UNP2(gt[0], gt[1], uu[0]); SB();
  ta[1][1] = LP(v4u, pa + 1280);
  cacc[3] = mfma(ta[0][1], tb[0][2], cacc[3]); SB();
  UNP2(gt[2], gt[3], uu[1]); SB();
  ta[1][2] = LP(v4u, pa + 768);
  cacc[2] = mfma(ta[0][2], tb[0][0], cacc[2]); SB();
  ADDK2(gu[0], gu[1], dsk[0]); SB();
  ta[1][3] = LP(v4u, pa + 256);
  cacc[3] = mfma(ta[0][2], tb[0][1], cacc[3]); SB();
  ADDK2(gu[2], gu[3], dsk[0]); SB();
  ta[1][4] = LP(v4u, pa + 3904);
  LDV(w1f[17], l16, w1b + 16384, 1024);
  cacc[3] = mfma(ta[0][3], tb[0][0], cacc[3]); SB();
  FMAC2B(cb[0][0], cb[0][1], gt[0], gt[1], gu[0], gu[1]); SB();
  tb[1][4] = LP(v4u, pbb + 2832);
  cacc[4] = mfma(ta[0][4], tb[0][4], cacc[4]); SB();
  FMAC2B(cb[0][2], cb[0][3], gt[2], gt[3], gu[2], gu[3]); SB();
  tb[1][5] = LP(v4u, pbb + 3344);
  cacc[5] = mfma(ta[0][4], tb[0][5], cacc[5]); SB();
  uu = LP(v2u, uep + 512); gu = LP(f32x4, xoff + 2048); SB();
  tb[1][6] = LP(v4u, pbb + 3856);
  cacc[6] = mfma(ta[0][4], tb[0][6], cacc[6]); SB();
  cb[1] = LP(f32x4, xoff + 2112); SB();
  tb[1][7] = LP(v4u, pbb + 4368);
  LDV(w1f[18], l16, w1b + 16384, 2048);
  cacc[7] = mfma(ta[0][4], tb[0][7], cacc[7]); SB();
  ta[1][5] = LP(v4u, pa + 3392);
  cacc[5] = mfma(ta[0][5], tb[0][4], cacc[5]); SB();
  ta[1][6] = LP(v4u, pa + 2880);
  cacc[6] = mfma(ta[0][5], tb[0][5], cacc[6]); SB();
  ta[1][7] = LP(v4u, pa + 2368);
  cacc[7] = mfma(ta[0][5], tb[0][6], cacc[7]); SB();
  UNP2(gt[0], gt[1], uu[0]); SB();
  LDV(w1f[19], l16, w1b + 16384, 3072);
  cacc[6] = mfma(ta[0][6], tb[0][4], cacc[6]); SB();
  UNP2(gt[2], gt[3], uu[1]); SB();
  cacc[7] = mfma(ta[0][6], tb[0][5], cacc[7]); SB();
  ADDK2(gu[0], gu[1], dsk[0]); SB();
  cacc[7] = mfma(ta[0][7], tb[0][4], cacc[7]); SB();
  ADDK2(gu[2], gu[3], dsk[0]); SB();
  ta[0][0] = LP(v4u, pa + 1728);
  cacc[0] = mfma(ta[1][0], tb[1][0], cacc[0]); SB();
  FMAC2B(cb[1][0], cb[1][1], gt[0], gt[1], gu[0], gu[1]); SB();
  LDV(w1f[20], l16, w1b + 20480, 0);
  cacc[1] = mfma(ta[1][0], tb[1][1], cacc[1]); SB();
  FMAC2B(cb[1][2], cb[1][3], gt[2], gt[3], gu[2], gu[3]); SB();
  tb[0][0] = LP(v4u, pbb + 192);
  cacc[2] = mfma(ta[1][0], tb[1][2], cacc[2]); SB();
  uu = LP(v2u, uep + 1024); gu = LP(f32x4, xoff + 4096); SB();
  tb[0][1] = LP(v4u, pbb + 704);
  cacc[3] = mfma(ta[1][0], tb[1][3], cacc[3]); SB();
  cb[2] = LP(f32x4, xoff + 4160); SB();
  tb[0][2] = LP(v4u, pbb + 1216);
  cacc[1] = mfma(ta[1][1], tb[1][0], cacc[1]); SB();
  tb[0][3] = LP(v4u, pbb + 1728);
  LDV(w1f[21], l16, w1b + 20480, 1024);
  cacc[2] = mfma(ta[1][1], tb[1][1], cacc[2]); SB();
  ta[0][1] = LP(v4u, pa + 1216);
  cacc[3] = mfma(ta[1][1], tb[1][2], cacc[3]); SB();
  ta[0][2] = LP(v4u, pa + 704);
  cacc[2] = mfma(ta[1][2], tb[1][0], cacc[2]); SB();
  UNP2(gt[0], gt[1], uu[0]); SB();
  ta[0][3] = LP(v4u, pa + 192);
  cacc[3] = mfma(ta[1][2], tb[1][1], cacc[3]); SB();
  UNP2(gt[2], gt[3], uu[1]); SB();
  ta[0][4] = LP(v4u, pa + 3840);
  LDV(w1f[22], l16, w1b + 20480, 2048);
  cacc[3] = mfma(ta[1][3], tb[1][0], cacc[3]); SB();
  ADDK2(gu[0], gu[1], dsk[0]); SB();
  tb[0][4] = LP(v4u, pbb + 2768);
  cacc[4] = mfma(ta[1][4], tb[1][4], cacc[4]); SB();
  ADDK2(gu[2], gu[3], dsk[0]); SB();
  tb[0][5] = LP(v4u, pbb + 3280);
  cacc[5] = mfma(ta[1][4], tb[1][5], cacc[5]); SB();
  FMAC2B(cb[2][0], cb[2][1], gt[0], gt[1], gu[0], gu[1]); SB();
  tb[0][6] = LP(v4u, pbb + 3792);
  cacc[6] = mfma(ta[1][4], tb[1][6], cacc[6]); SB();
  FMAC2B(cb[2][2], cb[2][3], gt[2], gt[3], gu[2], gu[3]); SB();
  tb[0][7] = LP(v4u, pbb + 4304);
  LDV(w1f[23], l16, w1b + 20480, 3072);
  cacc[7] = mfma(ta[1][4], tb[1][7], cacc[7]); SB();
  uu = LP(v2u, uep + 1536); gu = LP(f32x4, xoff + 6144); SB();
  ta[0][5] = LP(v4u, pa + 3328);
  cacc[5] = mfma(ta[1][5], tb[1][4], cacc[5]); SB();
  cb[3] = LP(f32x4, xoff + 6208); SB();
  ta[0][6] = LP(v4u, pa + 2816);
  cacc[6] = mfma(ta[1][5], tb[1][5], cacc[6]); SB();
  ta[0][7] = LP(v4u, pa + 2304);
  cacc[7] = mfma(ta[1][5], tb[1][6], cacc[7]); SB();
  LDV(w1f[24], l16, w1b + 24576, 0);
  cacc[6] = mfma(ta[1][6], tb[1][4], cacc[6]); SB();
  cacc[7] = mfma(ta[1][6], tb[1][5], cacc[7]); SB();
  UNP2(gt[0], gt[1], uu[0]); SB();
  cacc[7] = mfma(ta[1][7], tb[1][4], cacc[7]); SB();
  UNP2(gt[2], gt[3], uu[1]); SB();
  ta[1][0] = LP(v4u, pa + 1664);
  cacc[0] = mfma(ta[0][0], tb[0][0], cacc[0]); SB();
  ADDK2(gu[0], gu[1], dsk[0]); SB();
  LDA(w2f[0], l16, w2b + 0, 0);
  cacc[1] = mfma(ta[0][0], tb[0][1], cacc[1]); SB();
  ADDK2(gu[2], gu[3], dsk[0]); SB();
  tb[1][0] = LP(v4u, pbb + 128);
  cacc[2] = mfma(ta[0][0], tb[0][2], cacc[2]); SB();
  FMAC2B(cb[3][0], cb[3][1], gt[0], gt[1], gu[0], gu[1]); SB();
  tb[1][1] = LP(v4u, pbb + 640);
  cacc[3] = mfma(ta[0][0], tb[0][3], cacc[3]); SB();
  FMAC2B(cb[3][2], cb[3][3], gt[2], gt[3], gu[2], gu[3]); SB();
  tb[1][2] = LP(v4u, pbb + 1152);
  cacc[1] = mfma(ta[0][1], tb[0][0], cacc[1]); SB();
  uu = LP(v2u, uep + 2576); gu = LP(f32x4, xoff + 0); SB();
  tb[1][3] = LP(v4u, pbb + 1664);
  LDA(w2f[1], l16, w2b + 0, 1024);
  cacc[2] = mfma(ta[0][1], tb[0][1], cacc[2]); SB();
  cb[4] = LP(f32x4, xoff + 64); SB();
  ta[1][1] = LP(v4u, pa + 1152);
  cacc[3] = mfma(ta[0][1], tb[0][2], cacc[3]); SB();
  ta[1][2] = LP(v4u, pa + 640);
  cacc[2] = mfma(ta[0][2], tb[0][0], cacc[2]); SB();
  ta[1][3] = LP(v4u, pa + 128);
  cacc[3] = mfma(ta[0][2], tb[0][1], cacc[3]); SB();
  ta[1][4] = LP(v4u, pa + 3776);
  LDA(w2f[2], l16, w2b + 0, 2048);
  cacc[3] = mfma(ta[0][3], tb[0][0], cacc[3]); SB();
  UNP2(gt[0], gt[1], uu[0]); SB();
  tb[1][4] = LP(v4u, pbb + 2704);
  cacc[4] = mfma(ta[0][4], tb[0][4], cacc[4]); SB();
  UNP2(gt[2], gt[3], uu[1]); SB();
  tb[1][5] = LP(v4u, pbb + 3216);
  cacc[5] = mfma(ta[0][4], tb[0][5], cacc[5]); SB();
  ADDK2(gu[0], gu[1], dsk[1]); SB();
  tb[1][6] = LP(v4u, pbb + 3728);
  cacc[6] = mfma(ta[0][4], tb[0][6], cacc[6]); SB();
  PAD(8); SB();
  ADDK2(gu[2], gu[3], dsk[1]); SB();
  tb[1][7] = LP(v4u, pbb + 4240);
  LDA(w2f[3], l16, w2b + 0, 3072);
  cacc[7] = mfma(ta[0][4], tb[0][7], cacc[7]); SB();
  FMAC2B(cb[4][0], cb[4][1], gt[0], gt[1], gu[0], gu[1]); SB();
  ta[1][5] = LP(v4u, pa + 3264);
  cacc[5] = mfma(ta[0][5], tb[0][4], cacc[5]); SB();
  FMAC2B(cb[4][2], cb[4][3], gt[2], gt[3], gu[2], gu[3]); SB();
  ta[1][6] = LP(v4u, pa + 2752);
  cacc[6] = mfma(ta[0][5], tb[0][5], cacc[6]); SB();
  uu = LP(v2u, uep + 3088); gu = LP(f32x4, xoff + 2048); SB();
  ta[1][7] = LP(v4u, pa + 2240);
  cacc[7] = mfma(ta[0][5], tb[0][6], cacc[7]); SB();
  cb[5] = LP(f32x4, xoff + 2112); SB();
  LDA(w2f[4], l16, w2b + 4096, 0);
  cacc[6] = mfma(ta[0][6], tb[0][4], cacc[6]); SB();
  cacc[7] = mfma(ta[0][6], tb[0][5], cacc[7]); SB();
  cacc[7] = mfma(ta[0][7], tb[0][4], cacc[7]); SB();
  ta[0][0] = LP(v4u, pa + 1600);
  cacc[0] = mfma(ta[1][0], tb[1][0], cacc[0]); SB();
  UNP2(gt[0], gt[1], uu[0]); SB();
  LDA(w2f[5], l16, w2b + 4096, 1024);
  cacc[1] = mfma(ta[1][0], tb[1][1], cacc[1]); SB();
  UNP2(gt[2], gt[3], uu[1]); SB();
  tb[0][0] = LP(v4u, pbb + 64);
  cacc[2] = mfma(ta[1][0], tb[1][2], cacc[2]); SB();
  ADDK2(gu[0], gu[1], dsk[1]); SB();
  tb[0][1] = LP(v4u, pbb + 576);
  cacc[3] = mfma(ta[1][0], tb[1][3], cacc[3]); SB();
  ADDK2(gu[2], gu[3], dsk[1]); SB();
  tb[0][2] = LP(v4u, pbb + 1088);
  cacc[1] = mfma(ta[1][1], tb[1][0], cacc[1]); SB();
  FMAC2B(cb[5][0], cb[5][1], gt[0], gt[1], gu[0], gu[1]); SB();
  tb[0][3] = LP(v4u, pbb + 1600);
  LDA(w2f[6], l16, w2b + 4096, 2048);
  cacc[2] = mfma(ta[1][1], tb[1][1], cacc[2]); SB();
  FMAC2B(cb[5][2], cb[5][3], gt[2], gt[3], gu[2], gu[3]); SB();
  ta[0][1] = LP(v4u, pa + 1088);
  cacc[3] = mfma(ta[1][1], tb[1][2], cacc[3]); SB();
  uu = LP(v2u, uep + 3600); gu = LP(f32x4, xoff + 4096); SB();
  ta[0][2] = LP(v4u, pa + 576);
  cacc[2] = mfma(ta[1][2], tb[1][0], cacc[2]); SB();
  cb[6] = LP(f32x4, xoff + 4160); SB();
  ta[0][3] = LP(v4u, pa + 64);
  cacc[3] = mfma(ta[1][2], tb[1][1], cacc[3]); SB();
  ta[0][4] = LP(v4u, pa + 3712);
  LDA(w2f[7], l16, w2b + 4096, 3072);
  cacc[3] = mfma(ta[1][3], tb[1][0], cacc[3]); SB();
  tb[0][4] = LP(v4u, pbb + 2640);
  cacc[4] = mfma(ta[1][4], tb[1][4], cacc[4]); SB();
  tb[0][5] = LP(v4u, pbb + 3152);
  cacc[5] = mfma(ta[1][4], tb[1][5], cacc[5]); SB();
  UNP2(gt[0], gt[1], uu[0]); SB();
  tb[0][6] = LP(v4u, pbb + 3664);
  cacc[6] = mfma(ta[1][4], tb[1][6], cacc[6]); SB();
  UNP2(gt[2], gt[3], uu[1]); SB();
  tb[0][7] = LP(v4u, pbb + 4176);
  LDA(w2f[8], l16, w2b + 8192, 0);
  cacc[7] = mfma(ta[1][4], tb[1][7], cacc[7]); SB();
  ADDK2(gu[0], gu[1], dsk[1]); SB();
  ta[0][5] = LP(v4u, pa + 3200);
  cacc[5] = mfma(ta[1][5], tb[1][4], cacc[5]); SB();
  ADDK2(gu[2], gu[3], dsk[1]); SB();
  ta[0][6] = LP(v4u, pa + 2688);
  cacc[6] = mfma(ta[1][5], tb[1][5], cacc[6]); SB();
  FMAC2B(cb[6][0], cb[6][1], gt[0], gt[1], gu[0], gu[1]); SB();
  ta[0][7] = LP(v4u, pa + 2176);
  cacc[7] = mfma(ta[1][5], tb[1][6], cacc[7]); SB();
  FMAC2B(cb[6][2], cb[6][3], gt[2], gt[3], gu[2], gu[3]); SB();
  LDA(w2f[9], l16, w2b + 8192, 1024);
  cacc[6] = mfma(ta[1][6], tb[1][4], cacc[6]); SB();
  uu = LP(v2u, uep + 4112); gu = LP(f32x4, xoff + 6144); SB();
  cacc[7] = mfma(ta[1][6], tb[1][5], cacc[7]); SB();
  cb[7] = LP(f32x4, xoff + 6208); SB();
  cacc[7] = mfma(ta[1][7], tb[1][4], cacc[7]); SB();
  ta[1][0] = LP(v4u, pa + 1536);
  cacc[0] = mfma(ta[0][0], tb[0][0], cacc[0]); SB();
  LDA(w2f[10], l16, w2b + 8192, 2048);
  cacc[1] = mfma(ta[0][0], tb[0][1], cacc[1]); SB();
  tb[1][0] = LP(v4u, pbb + 0);
  cacc[2] = mfma(ta[0][0], tb[0][2], cacc[2]); SB();
  UNP2(gt[0], gt[1], uu[0]); SB();
  tb[1][1] = LP(v4u, pbb + 512);
  cacc[3] = mfma(ta[0][0], tb[0][3], cacc[3]); SB();
  UNP2(gt[2], gt[3], uu[1]); SB();
  tb[1][2] = LP(v4u, pbb + 1024);
  cacc[1] = mfma(ta[0][1], tb[0][0], cacc[1]); SB();
  ADDK2(gu[0], gu[1], dsk[1]); SB();
  tb[1][3] = LP(v4u, pbb + 1536);
  LDA(w2f[11], l16, w2b + 8192, 3072);
  cacc[2] = mfma(ta[0][1], tb[0][1], cacc[2]); SB();
  ADDK2(gu[2], gu[3], dsk[1]); SB();
  ta[1][1] = LP(v4u, pa + 1024);
  cacc[3] = mfma(ta[0][1], tb[0][2], cacc[3]); SB();
  FMAC2B(cb[7][0], cb[7][1], gt[0], gt[1], gu[0], gu[1]); SB();
  ta[1][2] = LP(v4u, pa + 512);
  cacc[2] = mfma(ta[0][2], tb[0][0], cacc[2]); SB();
  FMAC2B(cb[7][2], cb[7][3], gt[2], gt[3], gu[2], gu[3]); SB();
  ta[1][3] = LP(v4u, pa + 0);
  cacc[3] = mfma(ta[0][2], tb[0][1], cacc[3]); SB();
  ta[1][4] = LP(v4u, pa + 3648);
  LDA(w2f[12], l16, w2b + 12288, 0);
  cacc[3] = mfma(ta[0][3], tb[0][0], cacc[3]); SB();
  tb[1][4] = LP(v4u, pbb + 2576);
  cacc[4] = mfma(ta[0][4], tb[0][4], cacc[4]); SB();
  tb[1][5] = LP(v4u, pbb + 3088);
  cacc[5] = mfma(ta[0][4], tb[0][5], cacc[5]); SB();
  tb[1][6] = LP(v4u, pbb + 3600);
  cacc[6] = mfma(ta[0][4], tb[0][6], cacc[6]); SB();
  tb[1][7] = LP(v4u, pbb + 4112);
  LDA(w2f[13], l16, w2b + 12288, 1024);
  cacc[7] = mfma(ta[0][4], tb[0][7], cacc[7]); SB();
  ta[1][5] = LP(v4u, pa + 3136);
  cacc[5] = mfma(ta[0][5], tb[0][4], cacc[5]); SB();
  ta[1][6] = LP(v4u, pa + 2624);
  cacc[6] = mfma(ta[0][5], tb[0][5], cacc[6]); SB();
  ta[1][7] = LP(v4u, pa + 2112);
  cacc[7] = mfma(ta[0][5], tb[0][6], cacc[7]); SB();
  LDA(w2f[14], l16, w2b + 12288, 2048);
  cacc[6] = mfma(ta[0][6], tb[0][4], cacc[6]); SB();
  cacc[7] = mfma(ta[0][6], tb[0][5], cacc[7]); SB();
  cacc[7] = mfma(ta[0][7], tb[0][4], cacc[7]); SB();
  cacc[3] = mfma(ta[1][0], tb[1][3], cacc[3]); SB();
  LDA(w2f[15], l16, w2b + 12288, 3072);
  cacc[3] = mfma(ta[1][1], tb[1][2], cacc[3]); SB();
  cacc[3] = mfma(ta[1][2], tb[1][1], cacc[3]); SB();
  cacc[3] = mfma(ta[1][3], tb[1][0], cacc[3]); SB();
  cacc[7] = mfma(ta[1][4], tb[1][7], cacc[7]); SB();
  LDA(w2f[16], l16, w2b + 16384, 0);
  cacc[7] = mfma(ta[1][5], tb[1][6], cacc[7]); SB();
  cacc[7] = mfma(ta[1][6], tb[1][5], cacc[7]); SB();
  cacc[7] = mfma(ta[1][7], tb[1][4], cacc[7]); SB();
  cacc[2] = mfma(ta[1][0], tb[1][2], cacc[2]); SB();
  gt = cacc[3]; TIE(gt);
  ADD2(cb[3][0], cb[3][1], gt[0], gt[1]); SB();
  cacc[2] = mfma(ta[1][1], tb[1][1], cacc[2]); SB();
  ADD2(cb[3][2], cb[3][3], gt[2], gt[3]);
  CVT2(uu[0], uu[1], cb[3]); SB();
  cacc[2] = mfma(ta[1][2], tb[1][0], cacc[2]); SB();
  ST2(uu, hoff + 983040); SB();
  cacc[6] = mfma(ta[1][4], tb[1][6], cacc[6]); SB();
  cacc[6] = mfma(ta[1][5], tb[1][5], cacc[6]); SB();
  gt = cacc[7]; TIE(gt);
  ADD2(cb[7][0], cb[7][1], gt[0], gt[1]); SB();
  cacc[6] = mfma(ta[1][6], tb[1][4], cacc[6]); SB();
  ADD2(cb[7][2], cb[7][3], gt[2], gt[3]);
  CVT2(uu[0], uu[1], cb[7]); SB();
  cacc[1] = mfma(ta[1][0], tb[1][1], cacc[1]); SB();
  ST2(uu, hoff + 983168); SB();
  cacc[1] = mfma(ta[1][1], tb[1][0], cacc[1]); SB();
  gt = cacc[2]; TIE(gt);
  ADD2(cb[2][0], cb[2][1], gt[0], gt[1]); SB();
  cacc[5] = mfma(ta[1][4], tb[1][5], cacc[5]); SB();
  ADD2(cb[2][2], cb[2][3], gt[2], gt[3]);
  CVT2(uu[0], uu[1], cb[2]); SB();
  cacc[5] = mfma(ta[1][5], tb[1][4], cacc[5]); SB();
  ST2(uu, hoff + 655360); SB();
  cacc[0] = mfma(ta[1][0], tb[1][0], cacc[0]); SB();
  gt = cacc[6]; TIE(gt);
  ADD2(cb[6][0], cb[6][1], gt[0], gt[1]); SB();
  cacc[4] = mfma(ta[1][4], tb[1][4], cacc[4]); SB();
  ADD2(cb[6][2], cb[6][3], gt[2], gt[3]);
  CVT2(uu[0], uu[1], cb[6]); SB();
  ST2(uu, hoff + 655488); SB();
  cb[1] += cacc[1]; ST2((v2u{pk2(cb[1][0], cb[1][1]), pk2(cb[1][2], cb[1][3])}), hoff + 327680);
  cb[5] += cacc[5]; ST2((v2u{pk2(cb[5][0], cb[5][1]), pk2(cb[5][2], cb[5][3])}), hoff + 327808);
  cb[0] += cacc[0]; ST2((v2u{pk2(cb[0][0], cb[0][1]), pk2(cb[0][2], cb[0][3])}), hoff + 0);
  cb[4] += cacc[4]; ST2((v2u{pk2(cb[4][0], cb[4][1]), pk2(cb[4][2], cb[4][3])}), hoff + 128); SB();
  ADDR_B
  SB();
  GRID_PUBLISH
  asm volatile("; MARK_END");
  }
  SB();
  int w2c = w2b; asm volatile("" : "+s"(w2c));
  LDA(w2f[17], l16, w2c + 16384, 1024);
  cs1[0] = mfma(w1f[0], ones, Z4); SB();
  cs1[1] = mfma(w1f[1], ones, Z4); SB();
  LDA(w2f[18], l16, w2c + 16384, 2048);
  cs1[2] = mfma(w1f[2], ones, Z4); SB();
  cs1[3] = mfma(w1f[3], ones, Z4); SB();
  cs1[4] = mfma(w1f[4], ones, Z4); SB();
  LDA(w2f[19], l16, w2c + 16384, 3072);
  cs1[0] = mfma(w1f[5], ones, cs1[0]); SB();
  cs1[1] = mfma(w1f[6], ones, cs1[1]); SB();
  LDA(w2f[20], l16, w2c + 20480, 0);
  cs1[2] = mfma(w1f[7], ones, cs1[2]); SB();
  cs1[3] = mfma(w1f[8], ones, cs1[3]); SB();
  cs1[4] = mfma(w1f[9], ones, cs1[4]); SB();
  LDA(w2f[21], l16, w2c + 20480, 1024);
  cs1[0] = mfma(w1f[10], ones, cs1[0]); SB();
  cs1[1] = mfma(w1f[11], ones, cs1[1]); SB();
  LDA(w2f[22], l16, w2c + 20480, 2048);
  cs1[2] = mfma(w1f[12], ones, cs1[2]); SB();
  cs1[3] = mfma(w1f[13], ones, cs1[3]); SB();
  cs1[4] = mfma(w1f[14], ones, cs1[4]); SB();
  LDA(w2f[23], l16, w2c + 20480, 3072);
  cs1[0] = mfma(w1f[15], ones, cs1[0]); SB();
  cs1[1] = mfma(w1f[16], ones, cs1[1]); SB();
  LDA(w2f[24], l16, w2c + 24576, 0);
  cs1[2] = mfma(w1f[17], ones, cs1[2]); SB();
  cs1[3] = mfma(w1f[18], ones, cs1[3]); SB();
  cs1[4] = mfma(w1f[19], ones, cs1[4]); SB();
  cs1[0] = mfma(w1f[20], ones, cs1[0]); SB();
  cs1[1] = mfma(w1f[21], ones, cs1[1]); SB();
  cs1[2] = mfma(w1f[22], ones, cs1[2]); SB();
  cs1[3] = mfma(w1f[23], ones, cs1[3]); SB();
  cs1[4] = mfma(w1f[24], ones, cs1[4]); SB();
  ADDR_A
  SB();
  LP(f32x4, csw + 0) = cs1[0];
  LP(f32x4, csw + 64) = cs1[1];
  LP(f32x4, csw + 128) = cs1[2];
  LP(f32x4, csw + 192) = cs1[3];
  LP(f32x4, csw + 256) = cs1[4]; SB();
  wg_barrier(); wait_gridw(ctr + GF_OFF + ((bx & (GRP - 1)) * GSH + (pn < 16 ? 32 + w * 8 + (lane & 3) : w * 8 + (lane & 7))) * 32);
  dma_sc1(rs, ldsb + ML_TILE + w * 20480 + 0, l16, hb + 0);
  cs1[0] = (LP(f32x4, csr + 0) + LP(f32x4, csr + 320)) + (LP(f32x4, csr + 640) + LP(f32x4, csr + 960)); TIE(cs1[0]); SB();
  dma_sc1(rs, ldsb + ML_TILE + w * 20480 + 1024, l16, hb + 1024);
  cs1[1] = (LP(f32x4, csr + 64) + LP(f32x4, csr + 384)) + (LP(f32x4, csr + 704) + LP(f32x4, csr + 1024)); TIE(cs1[1]); SB();
  dma_sc1(rs, ldsb + ML_TILE + w * 20480 + 2048, l16, hb + 2048);
  cs1[2] = (LP(f32x4, csr + 128) + LP(f32x4, csr + 448)) + (LP(f32x4, csr + 768) + LP(f32x4, csr + 1088)); TIE(cs1[2]); SB();
  dma_sc1(rs, ldsb + ML_TILE + w * 20480 + 3072, l16, hb + 3072);
  cs1[3] = (LP(f32x4, csr + 192) + LP(f32x4, csr + 512)) + (LP(f32x4, csr + 832) + LP(f32x4, csr + 1152)); TIE(cs1[3]); SB();
  dma_sc1(rs, ldsb + ML_TILE + w * 20480 + 4096, l16, hb + 4096);
  cs1[4] = (LP(f32x4, csr + 256) + LP(f32x4, csr + 576)) + (LP(f32x4, csr + 896) + LP(f32x4, csr + 1216)); TIE(cs1[4]); SB();
  dma_sc1(rs, ldsb + ML_TILE + w * 20480 + 5120, l16, hb + 5120); SB();
  dma_sc1(rs, ldsb + ML_TILE + w * 20480 + 6144, l16, hb + 6144); SB();
  dma_sc1(rs, ldsb + ML_TILE + w * 20480 + 7168, l16, hb + 7168); SB();
  dma_sc1(rs, ldsb + ML_TILE + w * 20480 + 8192, l16, hb + 8192); SB();
  dma_sc1(rs, ldsb + ML_TILE + w * 20480 + 9216, l16, hb + 9216); SB();
  dma_sc1(rs, ldsb + ML_TILE + w * 20480 + 10240, l16, hb + 10240); SB();
  dma_sc1(rs, ldsb + ML_TILE + w * 20480 + 11264, l16, hb + 11264); SB();
  dma_sc1(rs, ldsb + ML_TILE + w * 20480 + 12288, l16, hb + 12288); SB();
  asm volatile("s_nop 0\n\ts_nop 0\n\ts_nop 0\n\ts_nop 0\n\ts_nop 0"); SB();
  dma_sc1(rs, ldsb + ML_TILE + w * 20480 + 13312, l16, hb + 13312); SB();
  PAD(2); SB();
  asm volatile("s_waitcnt vmcnt(10)" ::: "memory");
  { const v2u lo = trread(rp[0] + 0), hi = trread(rp[0] + 512); bf[0][0] = v4u{lo[0], lo[1], hi[0], hi[1]}; }
  { const v2u lo = trread(rp[1] + 0), hi = trread(rp[1] + 512); bf[0][1] = v4u{lo[0], lo[1], hi[0], hi[1]}; }
  { const v2u lo = trread(rp[2] + 0), hi = trread(rp[2] + 512); bf[0][2] = v4u{lo[0], lo[1], hi[0], hi[1]}; }
  { const v2u lo = trread(rp[3] + 0), hi = trread(rp[3] + 512); bf[0][3] = v4u{lo[0], lo[1], hi[0], hi[1]}; }
  SB();
  dma_sc1(rs, ldsb + ML_TILE + w * 20480 + 14336, l16, hb + 14336);
  dma_sc1(rs, ldsb + ML_TILE + w * 20480 + 15360, l16, hb + 15360);
  sa[0] = mfma(ones, bf[0][0], Z4); SB();
  qa[0] = mfma(bf[0][0], bf[0][0], Z4); SB();
  own[0] = mfma(w1f[0], bf[0][0], Z4); SB();
  own[1] = mfma(w1f[1], bf[0][0], Z4); SB();
  own[2] = mfma(w1f[2], bf[0][0], Z4); SB();
  own[3] = mfma(w1f[3], bf[0][0], Z4); SB();
  own[4] = mfma(w1f[4], bf[0][0], Z4); SB();
  dma_sc1(rs, ldsb + ML_TILE + w * 20480 + 16384, l16, hb + 16384);
  dma_sc1(rs, ldsb + ML_TILE + w * 20480 + 17408, l16, hb + 17408);
  asm volatile("s_waitcnt vmcnt(10)" ::: "memory");
  { const v2u lo = trread(rp[0] + 4096), hi = trread(rp[0] + 4608); bf[1][0] = v4u{lo[0], lo[1], hi[0], hi[1]}; }
  { const v2u lo = trread(rp[1] + 4096), hi = trread(rp[1] + 4608); bf[1][1] = v4u{lo[0], lo[1], hi[0], hi[1]}; }
  { const v2u lo = trread(rp[2] + 4096), hi = trread(rp[2] + 4608); bf[1][2] = v4u{lo[0], lo[1], hi[0], hi[1]}; }
  { const v2u lo = trread(rp[3] + 4096), hi = trread(rp[3] + 4608); bf[1][3] = v4u{lo[0], lo[1], hi[0], hi[1]}; }
  SB();
  sa[1] = mfma(ones, bf[0][1], Z4); SB();
  qa[1] = mfma(bf[0][1], bf[0][1], Z4); SB();
  ac1[0][0] = mfma(w1f[0], bf[0][1], Z4); SB();
  ac1[0][1] = mfma(w1f[1], bf[0][1], Z4); SB();
  ac1[0][2] = mfma(w1f[2], bf[0][1], Z4); SB();
  ac1[0][3] = mfma(w1f[3], bf[0][1], Z4); SB();
  ac1[0][4] = mfma(w1f[4], bf[0][1], Z4); SB();
  dma_sc1(rs, ldsb + ML_TILE + w * 20480 + 18432, l16, hb + 18432);
  dma_sc1(rs, ldsb + ML_TILE + w * 20480 + 19456, l16, hb + 19456);
  sa[2] = mfma(ones, bf[0][2], Z4); SB();
  qa[2] = mfma(bf[0][2], bf[0][2], Z4); SB();
  ac1[1][0] = mfma(w1f[0], bf[0][2], Z4); SB();
  ac1[1][1] = mfma(w1f[1], bf[0][2], Z4); SB();
  ac1[1][2] = mfma(w1f[2], bf[0][2], Z4); SB();
  ac1[1][3] = mfma(w1f[3], bf[0][2], Z4); SB();
  ac1[1][4] = mfma(w1f[4], bf[0][2], Z4); SB();
  LDA(w3f[0], l16, w3b + 0, 0);
  sa[3] = mfma(ones, bf[0][3], Z4); SB();
  qa[3] = mfma(bf[0][3], bf[0][3], Z4); SB();
  ac1[2][0] = mfma(w1f[0], bf[0][3], Z4); SB();
  ac1[2][1] = mfma(w1f[1], bf[0][3], Z4); SB();
  ac1[2][2] = mfma(w1f[2], bf[0][3], Z4); SB();
  ac1[2][3] = mfma(w1f[3], bf[0][3], Z4); SB();
  ac1[2][4] = mfma(w1f[4], bf[0][3], Z4); SB();
  LDA(w3f[1], l16, w3b + 0, 1024);
  sa[0] = mfma(ones, bf[1][0], sa[0]); SB();
  qa[0] = mfma(bf[1][0], bf[1][0], qa[0]); SB();
  own[0] = mfma(w1f[5], bf[1][0], own[0]); SB();
  own[1] = mfma(w1f[6], bf[1][0], own[1]); SB();
  own[2] = mfma(w1f[7], bf[1][0], own[2]); SB();
  own[3] = mfma(w1f[8], bf[1][0], own[3]); SB();
  own[4] = mfma(w1f[9], bf[1][0], own[4]); SB();
  LDA(w3f[2], l16, w3b + 0, 2048);
  asm volatile("s_waitcnt vmcnt(11)" ::: "memory");
  { const v2u lo = trread(rp[0] + 8192), hi = trread(rp[0] + 8704); bf[2][0] = v4u{lo[0], lo[1], hi[0], hi[1]}; }
  { const v2u lo = trread(rp[1] + 8192), hi = trread(rp[1] + 8704); bf[2][1] = v4u{lo[0], lo[1], hi[0], hi[1]}; }
  { const v2u lo = trread(rp[2] + 8192), hi = trread(rp[2] + 8704); bf[2][2] = v4u{lo[0], lo[1], hi[0], hi[1]}; }
  { const v2u lo = trread(rp[3] + 8192), hi = trread(rp[3] + 8704); bf[2][3] = v4u{lo[0], lo[1], hi[0], hi[1]}; }
  SB();
  sa[1] = mfma(ones, bf[1][1], sa[1]); SB();
  qa[1] = mfma(bf[1][1], bf[1][1], qa[1]); SB();
  ac1[0][0] = mfma(w1f[5], bf[1][1], ac1[0][0]); SB();
  ac1[0][1] = mfma(w1f[6], bf[1][1], ac1[0][1]); SB();
  ac1[0][2] = mfma(w1f[7], bf[1][1], ac1[0][2]); SB();
  ac1[0][3] = mfma(w1f[8], bf[1][1], ac1[0][3]); SB();
  ac1[0][4] = mfma(w1f[9], bf[1][1], ac1[0][4]); SB();
  LDA(w3f[3], l16, w3b + 0, 3072);
  sa[2] = mfma(ones, bf[1][2], sa[2]); SB();
  qa[2] = mfma(bf[1][2], bf[1][2], qa[2]); SB();
  ac1[1][0] = mfma(w1f[5], bf[1][2], ac1[1][0]); SB();
  ac1[1][1] = mfma(w1f[6], bf[1][2], ac1[1][1]); SB();
  ac1[1][2] = mfma(w1f[7], bf[1][2], ac1[1][2]); SB();
  ac1[1][3] = mfma(w1f[8], bf[1][2], ac1[1][3]); SB();
  ac1[1][4] = mfma(w1f[9], bf[1][2], ac1[1][4]); SB();
  LDA(w3f[4], l16, w3b + 4096, 0);
  sa[3] = mfma(ones, bf[1][3], sa[3]); SB();
  qa[3] = mfma(bf[1][3], bf[1][3], qa[3]); SB();
  ac1[2][0] = mfma(w1f[5], bf[1][3], ac1[2][0]); SB();
  ac1[2][1] = mfma(w1f[6], bf[1][3], ac1[2][1]); SB();
  ac1[2][2] = mfma(w1f[7], bf[1][3], ac1[2][2]); SB();
  ac1[2][3] = mfma(w1f[8], bf[1][3], ac1[2][3]); SB();
  ac1[2][4] = mfma(w1f[9], bf[1][3], ac1[2][4]); SB();
  LDA(w3f[5], l16, w3b + 4096, 1024);
  sa[0] = mfma(ones, bf[2][0], sa[0]); SB();
  qa[0] = mfma(bf[2][0], bf[2][0], qa[0]); SB();
  own[0] = mfma(w1f[10], bf[2][0], own[0]); SB();
  own[1] = mfma(w1f[11], bf[2][0], own[1]); SB();
  own[2] = mfma(w1f[12], bf[2][0], own[2]); SB();
  own[3] = mfma(w1f[13], bf[2][0], own[3]); SB();
  own[4] = mfma(w1f[14], bf[2][0], own[4]); SB();
  LDV(w3f[6], l16, w3b + 4096, 2048);
  asm volatile("s_waitcnt vmcnt(11)" ::: "memory");
  { const v2u lo = trread(rp[0] + 12288), hi = trread(rp[0] + 12800); bf[3][0] = v4u{lo[0], lo[1], hi[0], hi[1]}; }
  { const v2u lo = trread(rp[1] + 12288), hi = trread(rp[1] + 12800); bf[3][1] = v4u{lo[0], lo[1], hi[0], hi[1]}; }
  { const v2u lo = trread(rp[2] + 12288), hi = trread(rp[2] + 12800); bf[3][2] = v4u{lo[0], lo[1], hi[0], hi[1]}; }
  { const v2u lo = trread(rp[3] + 12288), hi = trread(rp[3] + 12800); bf[3][3] = v4u{lo[0], lo[1], hi[0], hi[1]}; }
  SB();
  sa[1] = mfma(ones, bf[2][1], sa[1]); SB();
  qa[1] = mfma(bf[2][1], bf[2][1], qa[1]); SB();
  ac1[0][0] = mfma(w1f[10], bf[2][1], ac1[0][0]); SB();
  ac1[0][1] = mfma(w1f[11], bf[2][1], ac1[0][1]); SB();
  ac1[0][2] = mfma(w1f[12], bf[2][1], ac1[0][2]); SB();
  ac1[0][3] = mfma(w1f[13], bf[2][1], ac1[0][3]); SB();
  ac1[0][4] = mfma(w1f[14], bf[2][1], ac1[0][4]); SB();
  LDV(w3f[7], l16, w3b + 4096, 3072);
  sa[2] = mfma(ones, bf[2][2], sa[2]); SB();
  qa[2] = mfma(bf[2][2], bf[2][2], qa[2]); SB();
  ac1[1][0] = mfma(w1f[10], bf[2][2], ac1[1][0]); SB();
  ac1[1][1] = mfma(w1f[11], bf[2][2], ac1[1][1]); SB();
  ac1[1][2] = mfma(w1f[12], bf[2][2], ac1[1][2]); SB();
  ac1[1][3] = mfma(w1f[13], bf[2][2], ac1[1][3]); SB();
  ac1[1][4] = mfma(w1f[14], bf[2][2], ac1[1][4]); SB();
  LDV(w3f[8], l16, w3b + 8192, 0);
  sa[3] = mfma(ones, bf[2][3], sa[3]); SB();
  qa[3] = mfma(bf[2][3], bf[2][3], qa[3]); SB();
  ac1[2][0] = mfma(w1f[10], bf[2][3], ac1[2][0]); SB();
  ac1[2][1] = mfma(w1f[11], bf[2][3], ac1[2][1]); SB();
  ac1[2][2] = mfma(w1f[12], bf[2][3], ac1[2][2]); SB();
  ac1[2][3] = mfma(w1f[13], bf[2][3], ac1[2][3]); SB();
  ac1[2][4] = mfma(w1f[14], bf[2][3], ac1[2][4]); SB();
  LDV(w3f[9], l16, w3b + 8192, 1024);
  LDV(w3f[10], l16, w3b + 8192, 2048);
  sa[0] = mfma(ones, bf[3][0], sa[0]); SB();
  qa[0] = mfma(bf[3][0], bf[3][0], qa[0]); SB();
  own[0] = mfma(w1f[15], bf[3][0], own[0]); SB();
  own[1] = mfma(w1f[16], bf[3][0], own[1]); SB();
  own[2] = mfma(w1f[17], bf[3][0], own[2]); SB();
  own[3] = mfma(w1f[18], bf[3][0], own[3]); SB();
  own[4] = mfma(w1f[19], bf[3][0], own[4]); SB();
  LDV(w3f[11], l16, w3b + 8192, 3072);
  LDV(w3f[12], l16, w3b + 12288, 0);
  asm volatile("s_waitcnt vmcnt(13)" ::: "memory");
  { const v2u lo = trread(rp[0] + 16384), hi = trread(rp[0] + 16896); bf[4][0] = v4u{lo[0], lo[1], hi[0], hi[1]}; }
  { const v2u lo = trread(rp[1] + 16384), hi = trread(rp[1] + 16896); bf[4][1] = v4u{lo[0], lo[1], hi[0], hi[1]}; }
  { const v2u lo = trread(rp[2] + 16384), hi = trread(rp[2] + 16896); bf[4][2] = v4u{lo[0], lo[1], hi[0], hi[1]}; }
  { const v2u lo = trread(rp[3] + 16384), hi = trread(rp[3] + 16896); bf[4][3] = v4u{lo[0], lo[1], hi[0], hi[1]}; }
  SB();
  sa[1] = mfma(ones, bf[3][1], sa[1]); SB();
  qa[1] = mfma(bf[3][1], bf[3][1], qa[1]); SB();
  ac1[0][0] = mfma(w1f[15], bf[3][1], ac1[0][0]); SB();
  ac1[0][1] = mfma(w1f[16], bf[3][1], ac1[0][1]); SB();
  ac1[0][2] = mfma(w1f[17], bf[3][1], ac1[0][2]); SB();
  ac1[0][3] = mfma(w1f[18], bf[3][1], ac1[0][3]); SB();
  ac1[0][4] = mfma(w1f[19], bf[3][1], ac1[0][4]); SB();
  LDV(w3f[13], l16, w3b + 12288, 1024);
  LDV(w3f[14], l16, w3b + 12288, 2048);
  sa[2] = mfma(ones, bf[3][2], sa[2]); SB();
  qa[2] = mfma(bf[3][2], bf[3][2], qa[2]); SB();
  ac1[1][0] = mfma(w1f[15], bf[3][2], ac1[1][0]); SB();
  ac1[1][1] = mfma(w1f[16], bf[3][2], ac1[1][1]); SB();
  ac1[1][2] = mfma(w1f[17], bf[3][2], ac1[1][2]); SB();
  ac1[1][3] = mfma(w1f[18], bf[3][2], ac1[1][3]); SB();
  ac1[1][4] = mfma(w1f[19], bf[3][2], ac1[1][4]); SB();
  LDV(w3f[15], l16, w3b + 12288, 3072);
  LDV(w3f[16], l16, w3b + 16384, 0);
  sa[3] = mfma(ones, bf[3][3], sa[3]); SB();
  qa[3] = mfma(bf[3][3], bf[3][3], qa[3]); SB();
  ac1[2][0] = mfma(w1f[15], bf[3][3], ac1[2][0]); SB();
  ac1[2][1] = mfma(w1f[16], bf[3][3], ac1[2][1]); SB();
  ac1[2][2] = mfma(w1f[17], bf[3][3], ac1[2][2]); SB();
  ac1[2][3] = mfma(w1f[18], bf[3][3], ac1[2][3]); SB();
  ac1[2][4] = mfma(w1f[19], bf[3][3], ac1[2][4]); SB();
  LDV(w3f[17], l16, w3b + 16384, 1024);
  LDV(w3f[18], l16, w3b + 16384, 2048);
  sa[1] = mfma(ones, bf[4][1], sa[1]); SB();
  qa[1] = mfma(bf[4][1], bf[4][1], qa[1]); SB();
  sa[0] = mfma(ones, bf[4][0], sa[0]); SB();
  qa[0] = mfma(bf[4][0], bf[4][0], qa[0]); SB();
  ac1[0][0] = mfma(w1f[20], bf[4][1], ac1[0][0]); SB();
  ac1[0][1] = mfma(w1f[21], bf[4][1], ac1[0][1]); SB();
  ac1[0][2] = mfma(w1f[22], bf[4][1], ac1[0][2]); SB();
  ac1[0][3] = mfma(w1f[23], bf[4][1], ac1[0][3]); SB();
  ac1[0][4] = mfma(w1f[24], bf[4][1], ac1[0][4]); SB();
  LDV(w3f[19], l16, w3b + 16384, 3072);
  LDV(w3f[20], l16, w3b + 20480, 0);
  sa[2] = mfma(ones, bf[4][2], sa[2]); SB();
  qa[2] = mfma(bf[4][2], bf[4][2], qa[2]); SB();
  LP(f32x4, stq + 256) = qa[1]; LP(float, sts + 64) = sa[1][0]; SB();
  ac1[1][0] = mfma(w1f[20], bf[4][2], ac1[1][0]); SB();
  LP(f32x4, redw + 0) = ac1[0][0]; SB();
  ac1[1][1] = mfma(w1f[21], bf[4][2], ac1[1][1]); SB();
  LP(f32x4, redw + 1024) = ac1[0][1]; SB();
  ac1[1][2] = mfma(w1f[22], bf[4][2], ac1[1][2]); SB();
  LP(f32x4, redw + 2048) = ac1[0][2]; SB();
  ac1[1][3] = mfma(w1f[23], bf[4][2], ac1[1][3]); SB();
  LP(f32x4, redw + 3072) = ac1[0][3]; SB();
  ac1[1][4] = mfma(w1f[24], bf[4][2], ac1[1][4]); SB();
  LP(f32x4, redw + 4096) = ac1[0][4]; SB();
  LP(f32x4, stq) = qa[0]; LP(float, sts) = sa[0][0]; SB();
  LDV(w3f[21], l16, w3b + 20480, 1024);
  sa[3] = mfma(ones, bf[4][3], sa[3]); SB();
  qa[3] = mfma(bf[4][3], bf[4][3], qa[3]); SB();
  LP(f32x4, stq + 512) = qa[2]; LP(float, sts + 128) = sa[2][0]; SB();
  ac1[2][0] = mfma(w1f[20], bf[4][3], ac1[2][0]); SB();
  LP(f32x4, redw + 5120) = ac1[1][0]; SB();
  ac1[2][1] = mfma(w1f[21], bf[4][3], ac1[2][1]); SB();
  LP(f32x4, redw + 6144) = ac1[1][1]; SB();
  ac1[2][2] = mfma(w1f[22], bf[4][3], ac1[2][2]); SB();
  LP(f32x4, redw + 7168) = ac1[1][2]; SB();
  ac1[2][3] = mfma(w1f[23], bf[4][3], ac1[2][3]); SB();
  LP(f32x4, redw + 8192) = ac1[1][3]; SB();
  ac1[2][4] = mfma(w1f[24], bf[4][3], ac1[2][4]); SB();
  LP(f32x4, redw + 9216) = ac1[1][4]; SB();
  own[0] = mfma(w1f[20], bf[4][0], own[0]); SB();
  LP(f32x4, stq + 768) = qa[3]; LP(float, sts + 192) = sa[3][0]; SB();
  own[1] = mfma(w1f[21], bf[4][0], own[1]); SB();
  LP(f32x4, redw + 10240) = ac1[2][0]; SB();
  own[2] = mfma(w1f[22], bf[4][0], own[2]); SB();
  LP(f32x4, redw + 11264) = ac1[2][1]; SB();
  own[3] = mfma(w1f[23], bf[4][0], own[3]); SB();
  LP(f32x4, redw + 12288) = ac1[2][2]; SB();
  own[4] = mfma(w1f[24], bf[4][0], own[4]); SB();
  LP(f32x4, redw + 13312) = ac1[2][3]; SB();
  LP(f32x4, redw + 14336) = ac1[2][4]; SB();
  wg_barrier();
  float sv[8] = {LP(float, str0), LP(float, stq0), LP(float, str1), LP(float, stq1), LP(float, str2), LP(float, stq2), LP(float, str3), LP(float, stq3)};
  p15[0] = LP(f32x4, redr[0] + 0);
  p15[5] = LP(f32x4, redr[1] + 0);
  p15[10] = LP(f32x4, redr[2] + 0);
  p15[1] = LP(f32x4, redr[0] + 1024);
  p15[6] = LP(f32x4, redr[1] + 1024);
  p15[11] = LP(f32x4, redr[2] + 1024);
  p15[2] = LP(f32x4, redr[0] + 2048);
  p15[7] = LP(f32x4, redr[1] + 2048);
  p15[12] = LP(f32x4, redr[2] + 2048);
  p15[3] = LP(f32x4, redr[0] + 3072);
  p15[8] = LP(f32x4, redr[1] + 3072);
  p15[13] = LP(f32x4, redr[2] + 3072);
  p15[4] = LP(f32x4, redr[0] + 4096);
  p15[9] = LP(f32x4, redr[1] + 4096);
  p15[14] = LP(f32x4, redr[2] + 4096); SB();
  TIE(own[0]);
  TIE(own[1]);
  TIE(own[2]);
  TIE(own[3]);
  TIE(own[4]);
  LDA(w3f[22], l16, w3b + 20480, 2048);
  LDA(w3f[23], l16, w3b + 20480, 3072); SB();
  m1 = ((sv[0] + sv[2]) + (sv[4] + sv[6])) * (1.0f / D);
  { const float var = fmaxf(((sv[1] + sv[3]) + (sv[5] + sv[7])) * (1.0f / D) - m1 * m1, 0.f); r1 = __builtin_amdgcn_rsqf(var + EPS); gk = gelu_k(m1, r1 * __builtin_amdgcn_rsqf(var * r1 * r1 + EPS)); }
  own[0] = (own[0] + p15[0]) + (p15[5] + p15[10]); TIE(own[0]);
  own[1] = (own[1] + p15[1]) + (p15[6] + p15[11]); TIE(own[1]);
  own[2] = (own[2] + p15[2]) + (p15[7] + p15[12]); TIE(own[2]);
  own[3] = (own[3] + p15[3]) + (p15[8] + p15[13]); TIE(own[3]);
  own[4] = (own[4] + p15[4]) + (p15[9] + p15[14]); TIE(own[4]); SB();
  LDA(w3f[24], l16, w3b + 24576, 0);
  LDA(w3f[25], l16, w3b + 24576, 1024); SB();
  ln_gelu_n<2>(&own[0], &cs1[0], gk, &o5[0]);
  ST4(o5[0], o5[1], g_p);
  ST4(o5[0], o5[1], g_hp); SB();
  LDA(w3f[26], l16, w3b + 24576, 2048);
  LDA(w3f[27], l16, w3b + 24576, 3072);
  LDA(w3f[28], l16, w3b + 28672, 0); SB();
  ln_gelu_n<3>(&own[2], &cs1[2], gk, &o5[2]);
  ST4(o5[2], o5[3], g_p + GS_REC);
  ST2(o5[4], g_s);
  ST4(o5[2], o5[3], g_hp + GS_REC);
  ST2(o5[4], g_hs); SB();
  LDA(w3f[29], l16, w3b + 28672, 1024);
  LDA(w3f[30], l16, w3b + 28672, 2048);
  LDA(w3f[31], l16, w3b + 28672, 3072);
  LDA(w3f[32], l16, w3b + 32768, 0);
  LDA(w3f[33], l16, w3b + 32768, 1024);
  LDA(w3f[34], l16, w3b + 32768, 2048); SB();
  asm volatile("s_waitcnt vmcnt(6)" ::: "memory");
  wg_barrier();
  SIGNAL(0); SB();
  asm volatile("s_branch 63\n\t.fill 63, 4, 0xbf800000"); SB();
  LDA(w3f[35], l16, w3b + 32768, 3072);
  LDA(w3f[36], l16, w3b + 36864, 0); SB();
  PAD(3); SB();
  ADDR_C
  hres[0] = trread(hro[0]);
  hres[1] = trread(hro[1]);
  hres[2] = trread(hro[2]);
  hres[3] = trread(hro[3]);
  hres[4] = trread(hro[4]);
  base[0] = f32x4{(bflo(hres[0][0]) - m1) * r1, (bfhi(hres[0][0]) - m1) * r1, (bflo(hres[0][1]) - m1) * r1, (bfhi(hres[0][1]) - m1) * r1}; TIE(base[0]);
  base[1] = f32x4{(bflo(hres[1][0]) - m1) * r1, (bfhi(hres[1][0]) - m1) * r1, (bflo(hres[1][1]) - m1) * r1, (bfhi(hres[1][1]) - m1) * r1}; TIE(base[1]);
  base[2] = f32x4{(bflo(hres[2][0]) - m1) * r1, (bfhi(hres[2][0]) - m1) * r1, (bflo(hres[2][1]) - m1) * r1, (bfhi(hres[2][1]) - m1) * r1}; TIE(base[2]);
  base[3] = f32x4{(bflo(hres[3][0]) - m1) * r1, (bfhi(hres[3][0]) - m1) * r1, (bflo(hres[3][1]) - m1) * r1, (bfhi(hres[3][1]) - m1) * r1}; TIE(base[3]);
  base[4] = f32x4{(bflo(hres[4][0]) - m1) * r1, (bfhi(hres[4][0]) - m1) * r1, (bflo(hres[4][1]) - m1) * r1, (bfhi(hres[4][1]) - m1) * r1}; TIE(base[4]); SB();
  wait_all(ctr + fl1);
  LDVS(af[0], a2v, ab2[0] + 0, 0);
  LDVS(af[1], a2v, ab2[0] + 0, 1280);
  LDVS(af[2], a2w, ab2[0] + 0, 2560);
  LDVS(af[3], a2v, ab2[0] + 3840, 0);
  LDVS(af[4], a2v, ab2[0] + 3840, 1280);
  LDVS(af[5], a2v, ab2[1] + 0, 0);
  LDVS(af[6], a2v, ab2[1] + 0, 1280);
  LDVS(af[7], a2w, ab2[1] + 0, 2560);
  LDVS(af[8], a2v, ab2[1] + 3840, 0);
  LDVS(af[9], a2v, ab2[1] + 3840, 1280);
  LDVS(af[10], a2v, ab2[2] + 0, 0);
  LDVS(af[11], a2v, ab2[2] + 0, 1280); SB();
  LDVS(af[12], a2w, ab2[2] + 0, 2560);
  asm volatile("s_waitcnt vmcnt(12)" : "+v"(af[0]) :: "memory");
  acc[0][0] = mfma(w2f[0], af[0], Z4); SB();
  acc[0][1] = mfma(w2f[1], af[0], Z4); SB();
  acc[0][2] = mfma(w2f[2], af[0], Z4); SB();
  acc[0][3] = mfma(w2f[3], af[0], Z4); SB();
  acc[0][4] = mfma(w2f[4], af[0], Z4); SB();
  LDVS(af[13], a2v, ab2[2] + 3840, 0);
  asm volatile("s_waitcnt vmcnt(12)" : "+v"(af[1]) :: "memory");
  acc[0][0] = mfma(w2f[5], af[1], acc[0][0]); SB();
  acc[0][1] = mfma(w2f[6], af[1], acc[0][1]); SB();
  acc[0][2] = mfma(w2f[7], af[1], acc[0][2]); SB();
  acc[0][3] = mfma(w2f[8], af[1], acc[0][3]); SB();
  acc[0][4] = mfma(w2f[9], af[1], acc[0][4]); SB();
  LDVS(af[0], a2v, ab2[2] + 3840, 1280);
  asm volatile("s_waitcnt vmcnt(12)" : "+v"(af[2]) :: "memory");
  acc[0][0] = mfma(w2f[10], af[2], acc[0][0]); SB();
  acc[0][1] = mfma(w2f[11], af[2], acc[0][1]); SB();
  acc[0][2] = mfma(w2f[12], af[2], acc[0][2]); SB();
  acc[0][3] = mfma(w2f[13], af[2], acc[0][3]); SB();
  acc[0][4] = mfma(w2f[14], af[2], acc[0][4]); SB();
  LDVS(af[1], a2v, ab2[3] + 0, 0);
  asm volatile("s_waitcnt vmcnt(12)" : "+v"(af[3]) :: "memory");
  acc[0][0] = mfma(w2f[15], af[3], acc[0][0]); SB();
  acc[0][1] = mfma(w2f[16], af[3], acc[0][1]); SB();
  acc[0][2] = mfma(w2f[17], af[3], acc[0][2]); SB();
  acc[0][3] = mfma(w2f[18], af[3], acc[0][3]); SB();
  acc[0][4] = mfma(w2f[19], af[3], acc[0][4]); SB();
  LDVS(af[2], a2v, ab2[3] + 0, 1280);
  asm volatile("s_waitcnt vmcnt(12)" : "+v"(af[4]) :: "memory");
  acc[0][0] = mfma(w2f[20], af[4], acc[0][0]); SB();
  acc[0][1] = mfma(w2f[21], af[4], acc[0][1]); SB();
  acc[0][2] = mfma(w2f[22], af[4], acc[0][2]); SB();
  acc[0][3] = mfma(w2f[23], af[4], acc[0][3]); SB();
  acc[0][4] = mfma(w2f[24], af[4], acc[0][4]); SB();
  LDVS(af[3], a2w, ab2[3] + 0, 2560);
  asm volatile("s_waitcnt vmcnt(12)" : "+v"(af[5]) :: "memory");
  acc[1][0] = mfma(w2f[0], af[5], Z4); SB();
  acc[1][1] = mfma(w2f[1], af[5], Z4); SB();
  LP(f32x4, redw + 0) = acc[0][0]; SB();
  acc[1][2] = mfma(w2f[2], af[5], Z4); SB();
  acc[1][3] = mfma(w2f[3], af[5], Z4); SB();
  acc[1][4] = mfma(w2f[4], af[5], Z4); SB();
  LDVS(af[4], a2v, ab2[3] + 3840, 0);
  asm volatile("s_waitcnt vmcnt(12)" : "+v"(af[6]) :: "memory");
  acc[1][0] = mfma(w2f[5], af[6], acc[1][0]); SB();
  LP(f32x4, redw + 1024) = acc[0][1]; SB();
  acc[1][1] = mfma(w2f[6], af[6], acc[1][1]); SB();
  acc[1][2] = mfma(w2f[7], af[6], acc[1][2]); SB();
  acc[1][3] = mfma(w2f[8], af[6], acc[1][3]); SB();
  acc[1][4] = mfma(w2f[9], af[6], acc[1][4]); SB();
  LP(f32x4, redw + 2048) = acc[0][2]; SB();
  LDVS(af[5], a2v, ab2[3] + 3840, 1280);
  asm volatile("s_waitcnt vmcnt(12)" : "+v"(af[7]) :: "memory");
  acc[1][0] = mfma(w2f[10], af[7], acc[1][0]); SB();
  acc[1][1] = mfma(w2f[11], af[7], acc[1][1]); SB();
  acc[1][2] = mfma(w2f[12], af[7], acc[1][2]); SB();
  acc[1][3] = mfma(w2f[13], af[7], acc[1][3]); SB();
  LP(f32x4, redw + 3072) = acc[0][3]; SB();
  acc[1][4] = mfma(w2f[14], af[7], acc[1][4]); SB();
  LDA(w3f[37], l16, w3b + 36864, 1024);
  asm volatile("s_waitcnt vmcnt(12)" : "+v"(af[8]) :: "memory");
  acc[1][0] = mfma(w2f[15], af[8], acc[1][0]); SB();
  acc[1][1] = mfma(w2f[16], af[8], acc[1][1]); SB();
  acc[1][2] = mfma(w2f[17], af[8], acc[1][2]); SB();
  LP(f32x4, redw + 4096) = acc[0][4]; SB();
  acc[1][3] = mfma(w2f[18], af[8], acc[1][3]); SB();
  acc[1][4] = mfma(w2f[19], af[8], acc[1][4]); SB();
  LDA(w3f[38], l16, w3b + 36864, 2048);
  asm volatile("s_waitcnt vmcnt(12)" : "+v"(af[9]) :: "memory");
  acc[1][0] = mfma(w2f[20], af[9], acc[1][0]); SB();
  acc[1][1] = mfma(w2f[21], af[9], acc[1][1]); SB();
  acc[1][2] = mfma(w2f[22], af[9], acc[1][2]); SB();
  acc[1][3] = mfma(w2f[23], af[9], acc[1][3]); SB();
  acc[1][4] = mfma(w2f[24], af[9], acc[1][4]); SB();
  LDA(w3f[39], l16, w3b + 36864, 3072);
  asm volatile("s_waitcnt vmcnt(12)" : "+v"(af[10]) :: "memory");
  acc[0][0] = mfma(w2f[0], af[10], Z4); SB();
  acc[0][1] = mfma(w2f[1], af[10], Z4); SB();
  LP(f32x4, redw + 5120) = acc[1][0]; SB();
  acc[0][2] = mfma(w2f[2], af[10], Z4); SB();
  acc[0][3] = mfma(w2f[3], af[10], Z4); SB();
  acc[0][4] = mfma(w2f[4], af[10], Z4); SB();
  LDA(w3f[40], l16, w3b + 40960, 0);
  asm volatile("s_waitcnt vmcnt(12)" : "+v"(af[11]) :: "memory");
  acc[0][0] = mfma(w2f[5], af[11], acc[0][0]); SB();
  LP(f32x4, redw + 6144) = acc[1][1]; SB();
  acc[0][1] = mfma(w2f[6], af[11], acc[0][1]); SB();
  acc[0][2] = mfma(w2f[7], af[11], acc[0][2]); SB();
  acc[0][3] = mfma(w2f[8], af[11], acc[0][3]); SB();
  acc[0][4] = mfma(w2f[9], af[11], acc[0][4]); SB();
  LP(f32x4, redw + 7168) = acc[1][2]; SB();
  LDA(w3f[41], l16, w3b + 40960, 1024);
  asm volatile("s_waitcnt vmcnt(12)" : "+v"(af[12]) :: "memory");
  acc[0][0] = mfma(w2f[10], af[12], acc[0][0]); SB();
  acc[0][1] = mfma(w2f[11], af[12], acc[0][1]); SB();
  acc[0][2] = mfma(w2f[12], af[12], acc[0][2]); SB();
  acc[0][3] = mfma(w2f[13], af[12], acc[0][3]); SB();
  LP(f32x4, redw + 8192) = acc[1][3]; SB();
  acc[0][4] = mfma(w2f[14], af[12], acc[0][4]); SB();
  LDA(w3f[42], l16, w3b + 40960, 2048);
  asm volatile("s_waitcnt vmcnt(12)" : "+v"(af[13]) :: "memory");
  acc[0][0] = mfma(w2f[15], af[13], acc[0][0]); SB();
  acc[0][1] = mfma(w2f[16], af[13], acc[0][1]); SB();
  acc[0][2] = mfma(w2f[17], af[13], acc[0][2]); SB();
  LP(f32x4, redw + 9216) = acc[1][4]; SB();
  acc[0][3] = mfma(w2f[18], af[13], acc[0][3]); SB();
  acc[0][4] = mfma(w2f[19], af[13], acc[0][4]); SB();
  asm volatile("s_waitcnt vmcnt(11)" : "+v"(af[0]) :: "memory");
  acc[0][0] = mfma(w2f[20], af[0], acc[0][0]); SB();
  acc[0][1] = mfma(w2f[21], af[0], acc[0][1]); SB();
  acc[0][2] = mfma(w2f[22], af[0], acc[0][2]); SB();
  acc[0][3] = mfma(w2f[23], af[0], acc[0][3]); SB();
  acc[0][4] = mfma(w2f[24], af[0], acc[0][4]); SB();
  asm volatile("s_waitcnt vmcnt(10)" : "+v"(af[1]) :: "memory");
  acc[1][0] = mfma(w2f[0], af[1], Z4); SB();
  acc[1][1] = mfma(w2f[1], af[1], Z4); SB();
  LP(f32x4, redw + 10240) = acc[0][0]; SB();
  acc[1][2] = mfma(w2f[2], af[1], Z4); SB();
  acc[1][3] = mfma(w2f[3], af[1], Z4); SB();
  LP(f32x4, redw + 11264) = acc[0][1]; SB();
  acc[1][4] = mfma(w2f[4], af[1], Z4); SB();
  asm volatile("s_waitcnt vmcnt(9)" : "+v"(af[2]) :: "memory");
  acc[1][0] = mfma(w2f[5], af[2], acc[1][0]); SB();
  LP(f32x4, redw + 12288) = acc[0][2]; SB();
  acc[1][1] = mfma(w2f[6], af[2], acc[1][1]); SB();
  acc[1][2] = mfma(w2f[7], af[2], acc[1][2]); SB();
  LP(f32x4, redw + 13312) = acc[0][3]; SB();
  acc[1][3] = mfma(w2f[8], af[2], acc[1][3]); SB();
  acc[1][4] = mfma(w2f[9], af[2], acc[1][4]); SB();
  LP(f32x4, redw + 14336) = acc[0][4]; SB();
  wg_barrier();
  p15[0] = LP(f32x4, redr[0] + 0);
  p15[1] = LP(f32x4, redr[0] + 1024);
  p15[2] = LP(f32x4, redr[0] + 2048);
  p15[3] = LP(f32x4, redr[0] + 3072);
  p15[4] = LP(f32x4, redr[0] + 4096);
  p15[5] = LP(f32x4, redr[1] + 0);
  p15[6] = LP(f32x4, redr[1] + 1024);
  p15[7] = LP(f32x4, redr[1] + 2048);
  p15[8] = LP(f32x4, redr[1] + 3072);
  p15[9] = LP(f32x4, redr[1] + 4096);
  p15[10] = LP(f32x4, redr[2] + 0);
  p15[11] = LP(f32x4, redr[2] + 1024);
  p15[12] = LP(f32x4, redr[2] + 2048);
  p15[13] = LP(f32x4, redr[2] + 3072);
  p15[14] = LP(f32x4, redr[2] + 4096);
  asm volatile("s_waitcnt vmcnt(8)" : "+v"(af[3]) :: "memory");
  acc[1][0] = mfma(w2f[10], af[3], acc[1][0]); SB();
  acc[1][1] = mfma(w2f[11], af[3], acc[1][1]); SB();
  acc[1][2] = mfma(w2f[12], af[3], acc[1][2]); SB();
  acc[1][3] = mfma(w2f[13], af[3], acc[1][3]); SB();
  acc[1][4] = mfma(w2f[14], af[3], acc[1][4]); SB();
  ADD2(base[0][0], base[0][1], p15[0][0], p15[0][1]); SB();
  asm volatile("s_waitcnt vmcnt(7)" : "+v"(af[4]) :: "memory");
  acc[1][0] = mfma(w2f[15], af[4], acc[1][0]); SB();
  ADD2(base[0][2], base[0][3], p15[0][2], p15[0][3]); SB();
  acc[1][1] = mfma(w2f[16], af[4], acc[1][1]); SB();
  ADD2(base[1][0], base[1][1], p15[1][0], p15[1][1]); SB();
  acc[1][2] = mfma(w2f[17], af[4], acc[1][2]); SB();
  ADD2(base[1][2], base[1][3], p15[1][2], p15[1][3]); SB();
  acc[1][3] = mfma(w2f[18], af[4], acc[1][3]); SB();
  ADD2(base[2][0], base[2][1], p15[2][0], p15[2][1]); SB();
  acc[1][4] = mfma(w2f[19], af[4], acc[1][4]); SB();
  ADD2(base[2][2], base[2][3], p15[2][2], p15[2][3]); SB();
  asm volatile("s_waitcnt vmcnt(6)" : "+v"(af[5]) :: "memory");
  acc[1][0] = mfma(w2f[20], af[5], acc[1][0]); SB();
  ADD2(base[3][0], base[3][1], p15[3][0], p15[3][1]); SB();
  acc[1][1] = mfma(w2f[21], af[5], acc[1][1]); SB();
  ADD2(base[3][2], base[3][3], p15[3][2], p15[3][3]); SB();
  acc[1][2] = mfma(w2f[22], af[5], acc[1][2]); SB();
  ADD2(base[4][0], base[4][1], p15[4][0], p15[4][1]); SB();
  acc[1][3] = mfma(w2f[23], af[5], acc[1][3]); SB();
  ADD2(base[4][2], base[4][3], p15[4][2], p15[4][3]); SB();
  acc[1][4] = mfma(w2f[24], af[5], acc[1][4]); SB();
  LDA(w3f[43], l16, w3b + 40960, 3072);
  base[0] += (p15[5] + p15[10]) + acc[1][0]; o5[0] = v2u{pk2(base[0][0], base[0][1]), pk2(base[0][2], base[0][3])};
  LDA(w3f[44], l16, w3b + 45056, 0);
  base[1] += (p15[6] + p15[11]) + acc[1][1]; o5[1] = v2u{pk2(base[1][0], base[1][1]), pk2(base[1][2], base[1][3])};
  ST4(o5[0], o5[1], x_p);
  LDA(w3f[45], l16, w3b + 45056, 1024);
  base[2] += (p15[7] + p15[12]) + acc[1][2]; o5[2] = v2u{pk2(base[2][0], base[2][1]), pk2(base[2][2], base[2][3])};
  LDA(w3f[46], l16, w3b + 45056, 2048);
  base[3] += (p15[8] + p15[13]) + acc[1][3]; o5[3] = v2u{pk2(base[3][0], base[3][1]), pk2(base[3][2], base[3][3])};
  ST4(o5[2], o5[3], x_p + 1024);
  LDA(w3f[47], l16, w3b + 45056, 3072);
  base[4] += (p15[9] + p15[14]) + acc[1][4]; o5[4] = v2u{pk2(base[4][0], base[4][1]), pk2(base[4][2], base[4][3])};
  ST2(o5[4], x_s); SB();
  { f32x4 a4 = (base[0] + base[1]) + (base[2] + base[3]) + base[4];
    f32x4 b4 = base[0] * base[0]; b4 = __builtin_elementwise_fma(base[1], base[1], b4); b4 = __builtin_elementwise_fma(base[2], base[2], b4); b4 = __builtin_elementwise_fma(base[3], base[3], b4); b4 = __builtin_elementwise_fma(base[4], base[4], b4);
    const float a = sum_g((a4[0] + a4[1]) + (a4[2] + a4[3])), b = sum_g((b4[0] + b4[1]) + (b4[2] + b4[3]));
    ST2((v2u{__float_as_uint(a), __float_as_uint(b)}), st2o); }
  SB();
  LDA(w3f[48], l16, w3b + 49152, 0);
  asm volatile("" : "+a"(w3f[0]), "+a"(w3f[1]), "+a"(w3f[2]));
  cs3a[0] = mfma(w3f[0], ones, Z4); SB();
  cs3a[1] = mfma(w3f[1], ones, Z4); SB();
  cs3a[2] = mfma(w3f[2], ones, Z4); SB();
  LDA(w3f[49], l16, w3b + 49152, 1024);
  asm volatile("" : "+a"(w3f[3]), "+a"(w3f[4]), "+a"(w3f[5]));
  cs3a[0] = mfma(w3f[3], ones, cs3a[0]); SB();
  cs3a[1] = mfma(w3f[4], ones, cs3a[1]); SB();
  cs3a[2] = mfma(w3f[5], ones, cs3a[2]); SB();
  LDA(w3f[50], l16, w3b + 49152, 2048);
  asm volatile("" : "+v"(w3f[6]), "+v"(w3f[7]), "+v"(w3f[8]));
  cs3a[0] = mfma(w3f[6], ones, cs3a[0]); SB();
  cs3a[1] = mfma(w3f[7], ones, cs3a[1]); SB();
  cs3a[2] = mfma(w3f[8], ones, cs3a[2]); SB();
  LDA(w3f[51], l16, w3b + 49152, 3072);
  asm volatile("" : "+v"(w3f[9]), "+v"(w3f[10]), "+v"(w3f[11]));
  cs3a[0] = mfma(w3f[9], ones, cs3a[0]); SB();
  cs3a[1] = mfma(w3f[10], ones, cs3a[1]); SB();
  cs3a[2] = mfma(w3f[11], ones, cs3a[2]); SB();
  LDA(w3f[52], l16, w3b + 53248, 0);
  asm volatile("" : "+v"(w3f[12]), "+v"(w3f[13]), "+v"(w3f[14]));
  cs3a[0] = mfma(w3f[12], ones, cs3a[0]); SB();
  cs3a[1] = mfma(w3f[13], ones, cs3a[1]); SB();
  cs3a[2] = mfma(w3f[14], ones, cs3a[2]); SB();
  LDA(w3f[53], l16, w3b + 53248, 1024);
  asm volatile("" : "+v"(w3f[15]), "+v"(w3f[16]), "+v"(w3f[17]));
  cs3a[0] = mfma(w3f[15], ones, cs3a[0]); SB();
  cs3a[1] = mfma(w3f[16], ones, cs3a[1]); SB();
  cs3a[2] = mfma(w3f[17], ones, cs3a[2]); SB();
  LDA(w3f[54], l16, w3b + 53248, 2048);
  asm volatile("" : "+v"(w3f[18]), "+v"(w3f[19]), "+v"(w3f[20]));
  cs3a[0] = mfma(w3f[18], ones, cs3a[0]); SB();
  cs3a[1] = mfma(w3f[19], ones, cs3a[1]); SB();
  cs3a[2] = mfma(w3f[20], ones, cs3a[2]); SB();
  asm volatile("s_waitcnt vmcnt(7)" ::: "memory");
  wg_barrier();
  SIGNAL(1); SB();
  base[0] = f32x4{bflo(hres[0][0]) + bflo(o5[0][0]), bfhi(hres[0][0]) + bfhi(o5[0][0]), bflo(hres[0][1]) + bflo(o5[0][1]), bfhi(hres[0][1]) + bfhi(o5[0][1])}; TIE(base[0]);
  LDA(w3f[55], l16, w3b + 53248, 3072); SB();
  base[1] = f32x4{bflo(hres[1][0]) + bflo(o5[1][0]), bfhi(hres[1][0]) + bfhi(o5[1][0]), bflo(hres[1][1]) + bflo(o5[1][1]), bfhi(hres[1][1]) + bfhi(o5[1][1])}; TIE(base[1]);
  LDA(w3f[56], l16, w3b + 57344, 0); SB();
  base[2] = f32x4{bflo(hres[2][0]) + bflo(o5[2][0]), bfhi(hres[2][0]) + bfhi(o5[2][0]), bflo(hres[2][1]) + bflo(o5[2][1]), bfhi(hres[2][1]) + bfhi(o5[2][1])}; TIE(base[2]);
  LDA(w3f[57], l16, w3b + 57344, 1024); SB();
  base[3] = f32x4{bflo(hres[3][0]) + bflo(o5[3][0]), bfhi(hres[3][0]) + bfhi(o5[3][0]), bflo(hres[3][1]) + bflo(o5[3][1]), bfhi(hres[3][1]) + bfhi(o5[3][1])}; TIE(base[3]);
  LDA(w3f[58], l16, w3b + 57344, 2048); SB();
  asm volatile("s_branch 21\n\t.fill 21, 4, 0xbf800000"); SB();
  base[4] = f32x4{bflo(hres[4][0]) + bflo(o5[4][0]), bfhi(hres[4][0]) + bfhi(o5[4][0]), bflo(hres[4][1]) + bflo(o5[4][1]), bfhi(hres[4][1]) + bfhi(o5[4][1])}; TIE(base[4]);
  LDA(w3f[59], l16, w3b + 57344, 3072); SB();
  PAD(4); SB();
  ADDR_D
  SB();
  asm volatile("" : "+v"(w3f[21]), "+a"(w3f[22]), "+a"(w3f[23]));
  cs3a[0] = mfma(w3f[21], ones, cs3a[0]); SB();
  cs3a[1] = mfma(w3f[22], ones, cs3a[1]); SB();
  cs3a[2] = mfma(w3f[23], ones, cs3a[2]); SB();
  asm volatile("" : "+a"(w3f[24]), "+a"(w3f[25]), "+a"(w3f[26]));
  cs3a[0] = mfma(w3f[24], ones, cs3a[0]); SB();
  cs3a[1] = mfma(w3f[25], ones, cs3a[1]); SB();
  cs3a[2] = mfma(w3f[26], ones, cs3a[2]); SB();
  wait_blk(ctr + 8192 + (zro >> 2));
  LDVS(stp, sto, WS_ST2, 0);
  dma_sc1(rs, ldsb + ML_TILE + w * 5120 + 0, l16, xb + 0);
  asm volatile("" : "+a"(w3f[27]), "+a"(w3f[28]), "+a"(w3f[29]));
  cs3a[0] = mfma(w3f[27], ones, cs3a[0]); SB();
  cs3a[1] = mfma(w3f[28], ones, cs3a[1]); SB();
  cs3a[2] = mfma(w3f[29], ones, cs3a[2]); SB();
  dma_sc1(rs, ldsb + ML_TILE + w * 5120 + 1024, l16, xb + 1024);
  asm volatile("" : "+a"(w3f[30]), "+a"(w3f[31]), "+a"(w3f[32]));
  cs3a[0] = mfma(w3f[30], ones, cs3a[0]); SB();
  cs3a[1] = mfma(w3f[31], ones, cs3a[1]); SB();
  cs3a[2] = mfma(w3f[32], ones, cs3a[2]); SB();
  dma_sc1(rs, ldsb + ML_TILE + w * 5120 + 2048, l16, xb + 2048);
  asm volatile("" : "+a"(w3f[33]), "+a"(w3f[34]), "+a"(w3f[35]));
  cs3a[0] = mfma(w3f[33], ones, cs3a[0]); SB();
  cs3a[1] = mfma(w3f[34], ones, cs3a[1]); SB();
  cs3a[2] = mfma(w3f[35], ones, cs3a[2]); SB();
  dma_sc1(rs, ldsb + ML_TILE + w * 5120 + 3072, l16, xb + 3072);
  asm volatile("" : "+a"(w3f[36]), "+a"(w3f[37]), "+a"(w3f[38]));
  cs3a[0] = mfma(w3f[36], ones, cs3a[0]); SB();
  cs3a[1] = mfma(w3f[37], ones, cs3a[1]); SB();
  cs3a[2] = mfma(w3f[38], ones, cs3a[2]); SB();
  dma_sc1(rs, ldsb + ML_TILE + w * 5120 + 4096, l16, xb + 4096);
  asm volatile("" : "+a"(w3f[39]), "+a"(w3f[40]), "+a"(w3f[41]));
  cs3a[0] = mfma(w3f[39], ones, cs3a[0]); SB();
  cs3a[1] = mfma(w3f[40], ones, cs3a[1]); SB();
  cs3a[2] = mfma(w3f[41], ones, cs3a[2]); SB();
  dma_sc1(rs, ldsb + ML_TILE + w * 5120 + 20480, l16, xb + 20480);
  asm volatile("" : "+a"(w3f[42]), "+a"(w3f[43]), "+a"(w3f[44]));
  cs3a[0] = mfma(w3f[42], ones, cs3a[0]); SB();
  cs3a[1] = mfma(w3f[43], ones, cs3a[1]); SB();
  cs3a[2] = mfma(w3f[44], ones, cs3a[2]); SB();
  dma_sc1(rs, ldsb + ML_TILE + w * 5120 + 21504, l16, xb + 21504);
  asm volatile("" : "+a"(w3f[45]), "+a"(w3f[46]), "+a"(w3f[47]));
  cs3a[0] = mfma(w3f[45], ones, cs3a[0]); SB();
  cs3a[1] = mfma(w3f[46], ones, cs3a[1]); SB();
  cs3a[2] = mfma(w3f[47], ones, cs3a[2]); SB();
  dma_sc1(rs, ldsb + ML_TILE + w * 5120 + 22528, l16, xb + 22528);
  asm volatile("s_waitcnt vmcnt(19)" : "+a"(w3f[48]), "+a"(w3f[49]), "+a"(w3f[50]) :: "memory");
  cs3a[0] = mfma(w3f[48], ones, cs3a[0]); SB();
  cs3a[1] = mfma(w3f[49], ones, cs3a[1]); SB();
  cs3a[2] = mfma(w3f[50], ones, cs3a[2]); SB();
  dma_sc1(rs, ldsb + ML_TILE + w * 5120 + 23552, l16, xb + 23552);
  asm volatile("s_waitcnt vmcnt(17)" : "+a"(w3f[51]), "+a"(w3f[52]), "+a"(w3f[53]) :: "memory");
  cs3a[0] = mfma(w3f[51], ones, cs3a[0]); SB();
  cs3a[1] = mfma(w3f[52], ones, cs3a[1]); SB();
  cs3a[2] = mfma(w3f[53], ones, cs3a[2]); SB();
  dma_sc1(rs, ldsb + ML_TILE + w * 5120 + 24576, l16, xb + 24576);
  asm volatile("s_waitcnt vmcnt(14)" : "+a"(w3f[54]), "+a"(w3f[55]), "+a"(w3f[56]) :: "memory");
  cs3a[0] = mfma(w3f[54], ones, cs3a[0]); SB();
  cs3a[1] = mfma(w3f[55], ones, cs3a[1]); SB();
  cs3a[2] = mfma(w3f[56], ones, cs3a[2]); SB();
  dma_sc1(rs, ldsb + ML_TILE + w * 5120 + 40960, l16, xb + 40960);
  asm volatile("s_waitcnt vmcnt(12)" : "+a"(w3f[57]), "+a"(w3f[58]), "+a"(w3f[59]) :: "memory");
  cs3a[0] = mfma(w3f[57], ones, cs3a[0]); SB();
  cs3a[1] = mfma(w3f[58], ones, cs3a[1]); SB();
  cs3a[2] = mfma(w3f[59], ones, cs3a[2]); SB();
  dma_sc1(rs, ldsb + ML_TILE + w * 5120 + 41984, l16, xb + 41984); SB();
  asm volatile("s_waitcnt vmcnt(12)" : "+v"(stp) :: "memory");
  p3_stat(sts, stp); SB();
  asm volatile("s_waitcnt vmcnt(7)" ::: "memory");
  wg_barrier();
  bq[0] = LP(v4u, xr + 0);
  bq[1] = LP(v4u, xr + 1024);
  bq[2] = LP(v4u, xr + 2048);
  bq[3] = LP(v4u, xr + 3072); SB();
  asm volatile("" :: "v"(bq[3]));
  bq[4] = LP(v4u, xr + 4096);
  bq[5] = LP(v4u, xr + 5120);
  MM0A(a3[0][0], w3f[0], bq[0]); SB();
  dma_sc1(rs, ldsb + ML_TILE + w * 5120 + 43008, l16, xb + 43008);
  MM0A(a3[0][1], w3f[1], bq[0]); SB();
  MM0A(a3[0][2], w3f[2], bq[0]); SB();
  bq[6] = LP(v4u, xr + 6144);
  bq[7] = LP(v4u, xr + 7168);
  MMA(a3[0][0], w3f[3], bq[1]); SB();
  MMA(a3[0][1], w3f[4], bq[1]); SB();
  dma_sc1(rs, ldsb + ML_TILE + w * 5120 + 44032, l16, xb + 44032);
  MMA(a3[0][2], w3f[5], bq[1]); SB();
  MMV(a3[0][0], w3f[6], bq[2]); SB();
  gk3[0] = LP(f32x4, strd + 0); SB();
  MMV(a3[0][1], w3f[7], bq[2]); SB();
  gk3[1] = LP(f32x4, strd + 256); SB();
  MMV(a3[0][2], w3f[8], bq[2]); SB();
  gk3[2] = LP(f32x4, strd + 512); SB();
  dma_sc1(rs, ldsb + ML_TILE + w * 5120 + 45056, l16, xb + 45056);
  MMV(a3[0][0], w3f[9], bq[3]); SB();
  gk3[3] = LP(f32x4, strd + 768); SB();
  MMV(a3[0][1], w3f[10], bq[3]); SB();
  RD2(cs3[0][0], cs3[0][1], cs3a[0][0], cs3a[0][1]); SB();
  MMV(a3[0][2], w3f[11], bq[3]); SB();
  RD2(cs3[0][2], cs3[0][3], cs3a[0][2], cs3a[0][3]); SB();
  asm volatile("" :: "v"(bq[7]));
  bq[0] = LP(v4u, xr + 8192);
  bq[1] = LP(v4u, xr + 9216);
  MMV(a3[0][0], w3f[12], bq[4]); SB();
  RD2(cs3[1][0], cs3[1][1], cs3a[1][0], cs3a[1][1]); SB();
  dma_sc1(rs, ldsb + ML_TILE + w * 5120 + 61440, l16, xb + 61440);
  MMV(a3[0][1], w3f[13], bq[4]); SB();
  RD2(cs3[1][2], cs3[1][3], cs3a[1][2], cs3a[1][3]); SB();
  MMV(a3[0][2], w3f[14], bq[4]); SB();
  RD2(cs3[2][0], cs3[2][1], cs3a[2][0], cs3a[2][1]); SB();
  bq[2] = LP(v4u, xr + 10240);
  bq[3] = LP(v4u, xr + 11264);
  MMV(a3[0][0], w3f[15], bq[5]); SB();
  RD2(cs3[2][2], cs3[2][3], cs3a[2][2], cs3a[2][3]); SB();
  MMV(a3[0][1], w3f[16], bq[5]); SB();
  dma_sc1(rs, ldsb + ML_TILE + w * 5120 + 62464, l16, xb + 62464);
  MMV(a3[0][2], w3f[17], bq[5]); SB();
  MMV(a3[0][0], w3f[18], bq[6]); SB();
  MMV(a3[0][1], w3f[19], bq[6]); SB();
  MMV(a3[0][2], w3f[20], bq[6]); SB();
  dma_sc1(rs, ldsb + ML_TILE + w * 5120 + 63488, l16, xb + 63488);
  MMV(a3[0][0], w3f[21], bq[7]); SB();
  MMA(a3[0][1], w3f[22], bq[7]); SB();
  MMA(a3[0][2], w3f[23], bq[7]); SB();
  asm volatile("" :: "v"(bq[3]));
  bq[4] = LP(v4u, xr + 12288);
  bq[5] = LP(v4u, xr + 13312);
  MMA(a3[0][0], w3f[24], bq[0]); SB();
  dma_sc1(rs, ldsb + ML_TILE + w * 5120 + 64512, l16, xb + 64512);
  MMA(a3[0][1], w3f[25], bq[0]); SB();
  MMA(a3[0][2], w3f[26], bq[0]); SB();
  bq[6] = LP(v4u, xr + 14336);
  bq[7] = LP(v4u, xr + 15360);
  MMA(a3[0][0], w3f[27], bq[1]); SB();
  MMA(a3[0][1], w3f[28], bq[1]); SB();
  dma_sc1(rs, ldsb + ML_TILE + w * 5120 + 65536, l16, xb + 65536);
  MMA(a3[0][2], w3f[29], bq[1]); SB();
  MMA(a3[0][0], w3f[30], bq[2]); SB();
  MMA(a3[0][1], w3f[31], bq[2]); SB();
  MMA(a3[0][2], w3f[32], bq[2]); SB();
  LDA(w4f[0], l16, w4b + 0, 0);
  MMA(a3[0][0], w3f[33], bq[3]); SB();
  MMA(a3[0][1], w3f[34], bq[3]); SB();
  MMA(a3[0][2], w3f[35], bq[3]); SB();
  asm volatile("" :: "v"(bq[7]));
  bq[0] = LP(v4u, xr + 16384);
  bq[1] = LP(v4u, xr + 17408);
  MMA(a3[0][0], w3f[36], bq[4]); SB();
  LDA(w4f[1], l16, w4b + 0, 1024);
  MMA(a3[0][1], w3f[37], bq[4]); SB();
  MMA(a3[0][2], w3f[38], bq[4]); SB();
  bq[2] = LP(v4u, xr + 18432);
  bq[3] = LP(v4u, xr + 19456);
  MMA(a3[0][0], w3f[39], bq[5]); SB();
  MMA(a3[0][1], w3f[40], bq[5]); SB();
  LDA(w4f[2], l16, w4b + 0, 2048);
  MMA(a3[0][2], w3f[41], bq[5]); SB();
  MMA(a3[0][0], w3f[42], bq[6]); SB();
  MMA(a3[0][1], w3f[43], bq[6]); SB();
  MMA(a3[0][2], w3f[44], bq[6]); SB();
  LDA(w4f[3], l16, w4b + 0, 3072);
  MMA(a3[0][0], w3f[45], bq[7]); SB();
  MMA(a3[0][1], w3f[46], bq[7]); SB();
  MMA(a3[0][2], w3f[47], bq[7]); SB();
  asm volatile("s_waitcnt vmcnt(14)" ::: "memory");
  wg_barrier();
  asm volatile("" :: "v"(bq[3]));
  bq[4] = LP(v4u, xr + 20480);
  bq[5] = LP(v4u, xr + 21504);
  MMA(a3[0][0], w3f[48], bq[0]); SB();
  LDA(w4f[4], l16, w4b + 4096, 0);
  MMA(a3[0][1], w3f[49], bq[0]); SB();
  MMA(a3[0][2], w3f[50], bq[0]); SB();
  bq[6] = LP(v4u, xr + 22528);
  bq[7] = LP(v4u, xr + 23552);
  MMA(a3[0][0], w3f[51], bq[1]); SB();
  MMA(a3[0][1], w3f[52], bq[1]); SB();
  LDA(w4f[5], l16, w4b + 4096, 1024);
  MMA(a3[0][2], w3f[53], bq[1]); SB();
  MMA(a3[0][0], w3f[54], bq[2]); SB();
  MMA(a3[0][1], w3f[55], bq[2]); SB();
  MMA(a3[0][2], w3f[56], bq[2]); SB();
  LDA(w4f[6], l16, w4b + 4096, 2048);
  MMA(a3[0][0], w3f[57], bq[3]); SB();
  MMA(a3[0][1], w3f[58], bq[3]); SB();
  MMA(a3[0][2], w3f[59], bq[3]); SB();
  asm volatile("" :: "v"(bq[7]));
  bq[0] = LP(v4u, xr + 24576);
  bq[1] = LP(v4u, xr + 25600);
  MM0A(a3[1][0], w3f[0], bq[4]); SB();
  LDA(w4f[7], l16, w4b + 4096, 3072);
  MM0A(a3[1][1], w3f[1], bq[4]); SB();
  MM0A(a3[1][2], w3f[2], bq[4]); SB();
  bq[2] = LP(v4u, xr + 26624);
  bq[3] = LP(v4u, xr + 27648);
  MMA(a3[1][0], w3f[3], bq[5]); SB();
  MMA(a3[1][1], w3f[4], bq[5]); SB();
  LDA(w4f[8], l16, w4b + 8192, 0);
  MMA(a3[1][2], w3f[5], bq[5]); SB();
  FMA2N(gt[0], gt[1], cs3[0][0], cs3[0][1], gk3[0][0], a3[0][0][0], a3[0][0][1]);
  FMA2N(gt[2], gt[3], cs3[0][2], cs3[0][3], gk3[0][0], a3[0][0][2], a3[0][0][3]); SB();
  MMV(a3[1][0], w3f[6], bq[6]); SB();
  MUL2(gu[0], gu[1], gt[0], gt[1], gt[0], gt[1]); SB();
  MMV(a3[1][1], w3f[7], bq[6]); SB();
  MUL2(gu[2], gu[3], gt[2], gt[3], gt[2], gt[3]); SB();
  MMV(a3[1][2], w3f[8], bq[6]); SB();
  FMA2(gu[0], gu[1], gk3[0][2], gk3[0][1]); SB();
  LDA(w4f[9], l16, w4b + 8192, 1024);
  MMV(a3[1][0], w3f[9], bq[7]); SB();
  FMA2(gu[2], gu[3], gk3[0][2], gk3[0][1]); SB();
  MMV(a3[1][1], w3f[10], bq[7]); SB();
  MUL2(gu[0], gu[1], gu[0], gu[1], gt[0], gt[1]);
  MUL2(gu[2], gu[3], gu[2], gu[3], gt[2], gt[3]); SB();
  MMV(a3[1][2], w3f[11], bq[7]); SB();
  EXP1(gu[0]); SB();
  asm volatile("" :: "v"(bq[3]));
  bq[4] = LP(v4u, xr + 28672);
  bq[5] = LP(v4u, xr + 29696);
  MMV(a3[1][0], w3f[12], bq[0]); SB();
  EXP1(gu[1]); SB();
  LDA(w4f[10], l16, w4b + 8192, 2048);
  MMV(a3[1][1], w3f[13], bq[0]); SB();
  EXP1(gu[2]); SB();
  MMV(a3[1][2], w3f[14], bq[0]); SB();
  EXP1(gu[3]); SB();
  bq[6] = LP(v4u, xr + 30720);
  bq[7] = LP(v4u, xr + 31744);
  MMV(a3[1][0], w3f[15], bq[1]); SB();
  FMA2(gu[0], gu[1], gk3[0][3], gk3[0][3]); SB();
  MMV(a3[1][1], w3f[16], bq[1]); SB();
  FMA2(gu[2], gu[3], gk3[0][3], gk3[0][3]);
  RCP1(gu[0]); SB();
  LDA(w4f[11], l16, w4b + 8192, 3072);
  MMV(a3[1][2], w3f[17], bq[1]); SB();
  RCP1(gu[1]); SB();
  MMV(a3[1][0], w3f[18], bq[2]); SB();
  RCP1(gu[2]); SB();
  MMV(a3[1][1], w3f[19], bq[2]); SB();
  RCP1(gu[3]); SB();
  MMV(a3[1][2], w3f[20], bq[2]); SB();
  MUL2(gu[0], gu[1], gu[0], gu[1], gt[0], gt[1]); SB();
  LDA(w4f[12], l16, w4b + 12288, 0);
  MMV(a3[1][0], w3f[21], bq[3]); SB();
  MUL2(gu[2], gu[3], gu[2], gu[3], gt[2], gt[3]);
  CVT2(o5[0][0], o5[0][1], gu); SB();
  MMA(a3[1][1], w3f[22], bq[3]); SB();
  FMA2N(gt[0], gt[1], cs3[1][0], cs3[1][1], gk3[0][0], a3[0][1][0], a3[0][1][1]); SB();
  MMA(a3[1][2], w3f[23], bq[3]); SB();
  FMA2N(gt[2], gt[3], cs3[1][2], cs3[1][3], gk3[0][0], a3[0][1][2], a3[0][1][3]); SB();
  asm volatile("" :: "v"(bq[7]));
  bq[0] = LP(v4u, xr + 32768);
  bq[1] = LP(v4u, xr + 33792);
  MMA(a3[1][0], w3f[24], bq[4]); SB();
  MUL2(gu[0], gu[1], gt[0], gt[1], gt[0], gt[1]); SB();
  LDA(w4f[13], l16, w4b + 12288, 1024);
  MMA(a3[1][1], w3f[25], bq[4]); SB();
  MUL2(gu[2], gu[3], gt[2], gt[3], gt[2], gt[3]); SB();
  MMA(a3[1][2], w3f[26], bq[4]); SB();
  FMA2(gu[0], gu[1], gk3[0][2], gk3[0][1]); SB();
  bq[2] = LP(v4u, xr + 34816);
  bq[3] = LP(v4u, xr + 35840);
  MMA(a3[1][0], w3f[27], bq[5]); SB();
  FMA2(gu[2], gu[3], gk3[0][2], gk3[0][1]);
  MUL2(gu[0], gu[1], gu[0], gu[1], gt[0], gt[1]); SB();
  MMA(a3[1][1], w3f[28], bq[5]); SB();
  MUL2(gu[2], gu[3], gu[2], gu[3], gt[2], gt[3]); SB();
  LDV(w4f[14], l16, w4b + 12288, 2048);
  MMA(a3[1][2], w3f[29], bq[5]); SB();
  EXP1(gu[0]); SB();
  MMA(a3[1][0], w3f[30], bq[6]); SB();
  EXP1(gu[1]); SB();
  MMA(a3[1][1], w3f[31], bq[6]); SB();
  EXP1(gu[2]); SB();
  MMA(a3[1][2], w3f[32], bq[6]); SB();
  EXP1(gu[3]);
  FMA2(gu[0], gu[1], gk3[0][3], gk3[0][3]); SB();
  LDV(w4f[15], l16, w4b + 12288, 3072);
  MMA(a3[1][0], w3f[33], bq[7]); SB();
  FMA2(gu[2], gu[3], gk3[0][3], gk3[0][3]); SB();
  MMA(a3[1][1], w3f[34], bq[7]); SB();
  RCP1(gu[0]); SB();
  MMA(a3[1][2], w3f[35], bq[7]); SB();
  RCP1(gu[1]); SB();
  asm volatile("" :: "v"(bq[3]));
  bq[4] = LP(v4u, xr + 36864);
  bq[5] = LP(v4u, xr + 37888);
  MMA(a3[1][0], w3f[36], bq[0]); SB();
  RCP1(gu[2]); SB();
  LDV(w4f[16], l16, w4b + 16384, 0);
  MMA(a3[1][1], w3f[37], bq[0]); SB();
  RCP1(gu[3]); SB();
  MMA(a3[1][2], w3f[38], bq[0]); SB();
  MUL2(gu[0], gu[1], gu[0], gu[1], gt[0], gt[1]);
  MUL2(gu[2], gu[3], gu[2], gu[3], gt[2], gt[3]); SB();
  bq[6] = LP(v4u, xr + 38912);
  bq[7] = LP(v4u, xr + 39936);
  MMA(a3[1][0], w3f[39], bq[1]); SB();
  CVT2(o5[1][0], o5[1][1], gu); SB();
  MMA(a3[1][1], w3f[40], bq[1]); SB();
  ST4(o5[0], o5[1], h_p + 0); SB();
  LDV(w4f[17], l16, w4b + 16384, 1024);
  MMA(a3[1][2], w3f[41], bq[1]); SB();
  FMA2N(gt[0], gt[1], cs3[2][0], cs3[2][1], gk3[0][0], a3[0][2][0], a3[0][2][1]); SB();
  asm volatile("s_waitcnt vmcnt(19)" ::: "memory");
  wg_barrier();
  MMA(a3[1][0], w3f[42], bq[2]); SB();
  FMA2N(gt[2], gt[3], cs3[2][2], cs3[2][3], gk3[0][0], a3[0][2][2], a3[0][2][3]); SB();
  MMA(a3[1][1], w3f[43], bq[2]); SB();
  MUL2(gu[0], gu[1], gt[0], gt[1], gt[0], gt[1]);
  MUL2(gu[2], gu[3], gt[2], gt[3], gt[2], gt[3]); SB();
  MMA(a3[1][2], w3f[44], bq[2]); SB();
  FMA2(gu[0], gu[1], gk3[0][2], gk3[0][1]); SB();
  LDV(w4f[18], l16, w4b + 16384, 2048);
  MMA(a3[1][0], w3f[45], bq[3]); SB();
  FMA2(gu[2], gu[3], gk3[0][2], gk3[0][1]); SB();
  MMA(a3[1][1], w3f[46], bq[3]); SB();
  MUL2(gu[0], gu[1], gu[0], gu[1], gt[0], gt[1]); SB();
  MMA(a3[1][2], w3f[47], bq[3]); SB();
  MUL2(gu[2], gu[3], gu[2], gu[3], gt[2], gt[3]); SB();
  asm volatile("" :: "v"(bq[7]));
  bq[0] = LP(v4u, xr + 40960);
  bq[1] = LP(v4u, xr + 41984);
  MMA(a3[1][0], w3f[48], bq[4]); SB();
  EXP1(gu[0]); SB();
  LDV(w4f[19], l16, w4b + 16384, 3072);
  MMA(a3[1][1], w3f[49], bq[4]); SB();
  EXP1(gu[1]);
  EXP1(gu[2]); SB();
  MMA(a3[1][2], w3f[50], bq[4]); SB();
  EXP1(gu[3]); SB();
  bq[2] = LP(v4u, xr + 43008);
  bq[3] = LP(v4u, xr + 44032);
  MMA(a3[1][0], w3f[51], bq[5]); SB();
  FMA2(gu[0], gu[1], gk3[0][3], gk3[0][3]); SB();
  MMA(a3[1][1], w3f[52], bq[5]); SB();
  FMA2(gu[2], gu[3], gk3[0][3], gk3[0][3]); SB();
  LDV(w4f[20], l16, w4b + 20480, 0);
  MMA(a3[1][2], w3f[53], bq[5]); SB();
  PAD(5); SB();
  RCP1(gu[0]); SB();
  MMA(a3[1][0], w3f[54], bq[6]); SB();
  RCP1(gu[1]);
  RCP1(gu[2]); SB();
  MMA(a3[1][1], w3f[55], bq[6]); SB();
  RCP1(gu[3]); SB();
  MMA(a3[1][2], w3f[56], bq[6]); SB();
  MUL2(gu[0], gu[1], gu[0], gu[1], gt[0], gt[1]); SB();
  LDV(w4f[21], l16, w4b + 20480, 1024);
  MMA(a3[1][0], w3f[57], bq[7]); SB();
  MUL2(gu[2], gu[3], gu[2], gu[3], gt[2], gt[3]); SB();
  MMA(a3[1][1], w3f[58], bq[7]); SB();
  CVT2(o5[2][0], o5[2][1], gu); SB();
  MMA(a3[1][2], w3f[59], bq[7]); SB();
  ST2(o5[2], h_s + 0); SB();
  asm volatile("" :: "v"(bq[3]));
  bq[4] = LP(v4u, xr + 45056);
  bq[5] = LP(v4u, xr + 46080);
  MM0A(a3[0][0], w3f[0], bq[0]); SB();
  LDV(w4f[22], l16, w4b + 20480, 2048);
  MM0A(a3[0][1], w3f[1], bq[0]); SB();
  MM0A(a3[0][2], w3f[2], bq[0]); SB();
  bq[6] = LP(v4u, xr + 47104);
  bq[7] = LP(v4u, xr + 48128);
  MMA(a3[0][0], w3f[3], bq[1]); SB();
  MMA(a3[0][1], w3f[4], bq[1]); SB();
  LDV(w4f[23], l16, w4b + 20480, 3072);
  MMA(a3[0][2], w3f[5], bq[1]); SB();
  FMA2N(gt[0], gt[1], cs3[0][0], cs3[0][1], gk3[1][0], a3[1][0][0], a3[1][0][1]);
  FMA2N(gt[2], gt[3], cs3[0][2], cs3[0][3], gk3[1][0], a3[1][0][2], a3[1][0][3]); SB();
  MMV(a3[0][0], w3f[6], bq[2]); SB();
  MUL2(gu[0], gu[1], gt[0], gt[1], gt[0], gt[1]); SB();
  MMV(a3[0][1], w3f[7], bq[2]); SB();
  MUL2(gu[2], gu[3], gt[2], gt[3], gt[2], gt[3]); SB();
  MMV(a3[0][2], w3f[8], bq[2]); SB();
  FMA2(gu[0], gu[1], gk3[1][2], gk3[1][1]); SB();
  LDV(w4f[24], l16, w4b + 24576, 0);
  MMV(a3[0][0], w3f[9], bq[3]); SB();
  FMA2(gu[2], gu[3], gk3[1][2], gk3[1][1]); SB();
  MMV(a3[0][1], w3f[10], bq[3]); SB();
  MUL2(gu[0], gu[1], gu[0], gu[1], gt[0], gt[1]);
  MUL2(gu[2], gu[3], gu[2], gu[3], gt[2], gt[3]); SB();
  MMV(a3[0][2], w3f[11], bq[3]); SB();
  EXP1(gu[0]); SB();
  asm volatile("" :: "v"(bq[7]));
  bq[0] = LP(v4u, xr + 49152);
  bq[1] = LP(v4u, xr + 50176);
  MMV(a3[0][0], w3f[12], bq[4]); SB();
  EXP1(gu[1]); SB();
  LDV(w4f[25], l16, w4b + 24576, 1024);
  MMV(a3[0][1], w3f[13], bq[4]); SB();
  EXP1(gu[2]); SB();
  MMV(a3[0][2], w3f[14], bq[4]); SB();
  EXP1(gu[3]); SB();
  bq[2] = LP(v4u, xr + 51200);
  bq[3] = LP(v4u, xr + 52224);
  MMV(a3[0][0], w3f[15], bq[5]); SB();
  FMA2(gu[0], gu[1], gk3[1][3], gk3[1][3]); SB();
  MMV(a3[0][1], w3f[16], bq[5]); SB();
  FMA2(gu[2], gu[3], gk3[1][3], gk3[1][3]);
  RCP1(gu[0]); SB();
  LDV(w4f[26], l16, w4b + 24576, 2048);
  MMV(a3[0][2], w3f[17], bq[5]); SB();
  RCP1(gu[1]); SB();
  MMV(a3[0][0], w3f[18], bq[6]); SB();
  RCP1(gu[2]); SB();
  MMV(a3[0][1], w3f[19], bq[6]); SB();
  RCP1(gu[3]); SB();
  MMV(a3[0][2], w3f[20], bq[6]); SB();
  MUL2(gu[0], gu[1], gu[0], gu[1], gt[0], gt[1]); SB();
  dma_plain(rs, ldsb + ML_RED + w * 15360 + 0, l16, w4b + 27648);
  MMV(a3[0][0], w3f[21], bq[7]); SB();
  MUL2(gu[2], gu[3], gu[2], gu[3], gt[2], gt[3]);
  CVT2(o5[0][0], o5[0][1], gu); SB();
  MMA(a3[0][1], w3f[22], bq[7]); SB();
  FMA2N(gt[0], gt[1], cs3[1][0], cs3[1][1], gk3[1][0], a3[1][1][0], a3[1][1][1]); SB();
  MMA(a3[0][2], w3f[23], bq[7]); SB();
  FMA2N(gt[2], gt[3], cs3[1][2], cs3[1][3], gk3[1][0], a3[1][1][2], a3[1][1][3]); SB();
  asm volatile("" :: "v"(bq[3]));
  bq[4] = LP(v4u, xr + 53248);
  bq[5] = LP(v4u, xr + 54272);
  MMA(a3[0][0], w3f[24], bq[0]); SB();
  MUL2(gu[0], gu[1], gt[0], gt[1], gt[0], gt[1]); SB();
  dma_plain(rs, ldsb + ML_RED + w * 15360 + 1024, l16, w4b + 28672);
  MMA(a3[0][1], w3f[25], bq[0]); SB();
  MUL2(gu[2], gu[3], gt[2], gt[3], gt[2], gt[3]); SB();
  MMA(a3[0][2], w3f[26], bq[0]); SB();
  FMA2(gu[0], gu[1], gk3[1][2], gk3[1][1]); SB();
  bq[6] = LP(v4u, xr + 55296);
  bq[7] = LP(v4u, xr + 56320);
  MMA(a3[0][0], w3f[27], bq[1]); SB();
  FMA2(gu[2], gu[3], gk3[1][2], gk3[1][1]);
  MUL2(gu[0], gu[1], gu[0], gu[1], gt[0], gt[1]); SB();
  MMA(a3[0][1], w3f[28], bq[1]); SB();
  MUL2(gu[2], gu[3], gu[2], gu[3], gt[2], gt[3]); SB();
  dma_plain(rs, ldsb + ML_RED + w * 15360 + 2048, l16, w4b + 29696);
  MMA(a3[0][2], w3f[29], bq[1]); SB();
  EXP1(gu[0]); SB();
  MMA(a3[0][0], w3f[30], bq[2]); SB();
  EXP1(gu[1]); SB();
  MMA(a3[0][1], w3f[31], bq[2]); SB();
  EXP1(gu[2]); SB();
  MMA(a3[0][2], w3f[32], bq[2]); SB();
  EXP1(gu[3]);
  FMA2(gu[0], gu[1], gk3[1][3], gk3[1][3]); SB();
  dma_plain(rs, ldsb + ML_RED + w * 15360 + 3072, l16, w4b + 30720);
  MMA(a3[0][0], w3f[33], bq[3]); SB();
  FMA2(gu[2], gu[3], gk3[1][3], gk3[1][3]); SB();
  MMA(a3[0][1], w3f[34], bq[3]); SB();
  RCP1(gu[0]); SB();
  MMA(a3[0][2], w3f[35], bq[3]); SB();
  RCP1(gu[1]); SB();
  asm volatile("" :: "v"(bq[7]));
  bq[0] = LP(v4u, xr + 57344);
  bq[1] = LP(v4u, xr + 58368);
  MMA(a3[0][0], w3f[36], bq[4]); SB();
  RCP1(gu[2]); SB();
  dma_plain(rs, ldsb + ML_RED + w * 15360 + 4096, l16, w4b + 31744);
  MMA(a3[0][1], w3f[37], bq[4]); SB();
  RCP1(gu[3]); SB();
  MMA(a3[0][2], w3f[38], bq[4]); SB();
  MUL2(gu[0], gu[1], gu[0], gu[1], gt[0], gt[1]);
  MUL2(gu[2], gu[3], gu[2], gu[3], gt[2], gt[3]); SB();
  bq[2] = LP(v4u, xr + 59392);
  bq[3] = LP(v4u, xr + 60416);
  MMA(a3[0][0], w3f[39], bq[5]); SB();
  CVT2(o5[1][0], o5[1][1], gu); SB();
  MMA(a3[0][1], w3f[40], bq[5]); SB();
  ST4(o5[0], o5[1], h_p + 49152); SB();
  dma_plain(rs, ldsb + ML_RED + w * 15360 + 5120, l16, w4b + 32768);
  MMA(a3[0][2], w3f[41], bq[5]); SB();
  FMA2N(gt[0], gt[1], cs3[2][0], cs3[2][1], gk3[1][0], a3[1][2][0], a3[1][2][1]); SB();
  MMA(a3[0][0], w3f[42], bq[6]); SB();
  FMA2N(gt[2], gt[3], cs3[2][2], cs3[2][3], gk3[1][0], a3[1][2][2], a3[1][2][3]); SB();
  MMA(a3[0][1], w3f[43], bq[6]); SB();
  MUL2(gu[0], gu[1], gt[0], gt[1], gt[0], gt[1]);
  MUL2(gu[2], gu[3], gt[2], gt[3], gt[2], gt[3]); SB();
  MMA(a3[0][2], w3f[44], bq[6]); SB();
  FMA2(gu[0], gu[1], gk3[1][2], gk3[1][1]); SB();
  dma_plain(rs, ldsb + ML_RED + w * 15360 + 6144, l16, w4b + 33792);
  MMA(a3[0][0], w3f[45], bq[7]); SB();
  FMA2(gu[2], gu[3], gk3[1][2], gk3[1][1]); SB();
  MMA(a3[0][1], w3f[46], bq[7]); SB();
  MUL2(gu[0], gu[1], gu[0], gu[1], gt[0], gt[1]); SB();
  MMA(a3[0][2], w3f[47], bq[7]); SB();
  MUL2(gu[2], gu[3], gu[2], gu[3], gt[2], gt[3]); SB();
  asm volatile("" :: "v"(bq[3]));
  bq[4] = LP(v4u, xr + 61440);
  bq[5] = LP(v4u, xr + 62464);
  MMA(a3[0][0], w3f[48], bq[0]); SB();
  EXP1(gu[0]); SB();
  dma_plain(rs, ldsb + ML_RED + w * 15360 + 7168, l16, w4b + 34816);
  MMA(a3[0][1], w3f[49], bq[0]); SB();
  EXP1(gu[1]);
  EXP1(gu[2]); SB();
  MMA(a3[0][2], w3f[50], bq[0]); SB();
  EXP1(gu[3]); SB();
  bq[6] = LP(v4u, xr + 63488);
  bq[7] = LP(v4u, xr + 64512);
  MMA(a3[0][0], w3f[51], bq[1]); SB();
  FMA2(gu[0], gu[1], gk3[1][3], gk3[1][3]); SB();
  MMA(a3[0][1], w3f[52], bq[1]); SB();
  FMA2(gu[2], gu[3], gk3[1][3], gk3[1][3]); SB();
  dma_plain(rs, ldsb + ML_RED + w * 15360 + 8192, l16, w4b + 35840);
  MMA(a3[0][2], w3f[53], bq[1]); SB();
  RCP1(gu[0]); SB();
  MMA(a3[0][0], w3f[54], bq[2]); SB();
  RCP1(gu[1]);
  RCP1(gu[2]); SB();
  MMA(a3[0][1], w3f[55], bq[2]); SB();
  RCP1(gu[3]); SB();
  MMA(a3[0][2], w3f[56], bq[2]); SB();
  MUL2(gu[0], gu[1], gu[0], gu[1], gt[0], gt[1]); SB();
  dma_plain(rs, ldsb + ML_RED + w * 15360 + 9216, l16, w4b + 36864);
  MMA(a3[0][0], w3f[57], bq[3]); SB();
  MUL2(gu[2], gu[3], gu[2], gu[3], gt[2], gt[3]); SB();
  MMA(a3[0][1], w3f[58], bq[3]); SB();
  CVT2(o5[2][0], o5[2][1], gu); SB();
  MMA(a3[0][2], w3f[59], bq[3]); SB();
  ST2(o5[2], h_s + 49152); SB();
  asm volatile("" :: "v"(bq[7]));
  bq[0] = LP(v4u, xr + 65536);
  bq[1] = LP(v4u, xr + 66560);
  MM0A(a3[1][0], w3f[0], bq[4]); SB();
  dma_plain(rs, ldsb + ML_RED + w * 15360 + 10240, l16, w4b + 37888);
  MM0A(a3[1][1], w3f[1], bq[4]); SB();
  MM0A(a3[1][2], w3f[2], bq[4]); SB();
  bq[2] = LP(v4u, xr + 67584);
  bq[3] = LP(v4u, xr + 68608);
  MMA(a3[1][0], w3f[3], bq[5]); SB();
  dma_plain(rs, ldsb + ML_RED + w * 15360 + 11264, l16, w4b + 38912);
  MMA(a3[1][1], w3f[4], bq[5]); SB();
  MMA(a3[1][2], w3f[5], bq[5]); SB();
  FMA2N(gt[0], gt[1], cs3[0][0], cs3[0][1], gk3[2][0], a3[0][0][0], a3[0][0][1]);
  FMA2N(gt[2], gt[3], cs3[0][2], cs3[0][3], gk3[2][0], a3[0][0][2], a3[0][0][3]); SB();
  MMV(a3[1][0], w3f[6], bq[6]); SB();
  MUL2(gu[0], gu[1], gt[0], gt[1], gt[0], gt[1]); SB();
  dma_plain(rs, ldsb + ML_RED + w * 15360 + 12288, l16, w4b + 39936);
  MMV(a3[1][1], w3f[7], bq[6]); SB();
  MUL2(gu[2], gu[3], gt[2], gt[3], gt[2], gt[3]); SB();
  MMV(a3[1][2], w3f[8], bq[6]); SB();
  FMA2(gu[0], gu[1], gk3[2][2], gk3[2][1]); SB();
  MMV(a3[1][0], w3f[9], bq[7]); SB();
  FMA2(gu[2], gu[3], gk3[2][2], gk3[2][1]); SB();
  dma_plain(rs, ldsb + ML_RED + w * 15360 + 13312, l16, w4b + 40960);
  MMV(a3[1][1], w3f[10], bq[7]); SB();
  MUL2(gu[0], gu[1], gu[0], gu[1], gt[0], gt[1]);
  MUL2(gu[2], gu[3], gu[2], gu[3], gt[2], gt[3]); SB();
  MMV(a3[1][2], w3f[11], bq[7]); SB();
  EXP1(gu[0]); SB();
  asm volatile("" :: "v"(bq[3]));
  bq[4] = LP(v4u, xr + 69632);
  bq[5] = LP(v4u, xr + 70656);
  MMV(a3[1][0], w3f[12], bq[0]); SB();
  EXP1(gu[1]); SB();
  dma_plain(rs, ldsb + ML_RED + w * 15360 + 14336, l16, w4b + 41984);
  MMV(a3[1][1], w3f[13], bq[0]); SB();
  EXP1(gu[2]); SB();
  MMV(a3[1][2], w3f[14], bq[0]); SB();
  EXP1(gu[3]); SB();
  bq[6] = LP(v4u, xr + 71680);
  bq[7] = LP(v4u, xr + 72704);
  MMV(a3[1][0], w3f[15], bq[1]); SB();
  FMA2(gu[0], gu[1], gk3[2][3], gk3[2][3]); SB();
  LDA(w4f[42], l16, w4b + 40960, 2048);
  MMV(a3[1][1], w3f[16], bq[1]); SB();
  FMA2(gu[2], gu[3], gk3[2][3], gk3[2][3]);
  RCP1(gu[0]); SB();
  MMV(a3[1][2], w3f[17], bq[1]); SB();
  RCP1(gu[1]); SB();
  MMV(a3[1][0], w3f[18], bq[2]); SB();
  RCP1(gu[2]); SB();
  LDA(w4f[43], l16, w4b + 40960, 3072);
  MMV(a3[1][1], w3f[19], bq[2]); SB();
  RCP1(gu[3]); SB();
  MMV(a3[1][2], w3f[20], bq[2]); SB();
  MUL2(gu[0], gu[1], gu[0], gu[1], gt[0], gt[1]); SB();
  MMV(a3[1][0], w3f[21], bq[3]); SB();
  MUL2(gu[2], gu[3], gu[2], gu[3], gt[2], gt[3]);
  CVT2(o5[0][0], o5[0][1], gu); SB();
  LDA(w4f[44], l16, w4b + 45056, 0);
  MMA(a3[1][1], w3f[22], bq[3]); SB();
  FMA2N(gt[0], gt[1], cs3[1][0], cs3[1][1], gk3[2][0], a3[0][1][0], a3[0][1][1]); SB();
  MMA(a3[1][2], w3f[23], bq[3]); SB();
  FMA2N(gt[2], gt[3], cs3[1][2], cs3[1][3], gk3[2][0], a3[0][1][2], a3[0][1][3]); SB();
  asm volatile("" :: "v"(bq[7]));
  bq[0] = LP(v4u, xr + 73728);
  bq[1] = LP(v4u, xr + 74752);
  MMA(a3[1][0], w3f[24], bq[4]); SB();
  MUL2(gu[0], gu[1], gt[0], gt[1], gt[0], gt[1]); SB();
  LDA(w4f[45], l16, w4b + 45056, 1024);
  MMA(a3[1][1], w3f[25], bq[4]); SB();
  MUL2(gu[2], gu[3], gt[2], gt[3], gt[2], gt[3]); SB();
  MMA(a3[1][2], w3f[26], bq[4]); SB();
  FMA2(gu[0], gu[1], gk3[2][2], gk3[2][1]); SB();
  bq[2] = LP(v4u, xr + 75776);
  bq[3] = LP(v4u, xr + 76800);
  MMA(a3[1][0], w3f[27], bq[5]); SB();
  FMA2(gu[2], gu[3], gk3[2][2], gk3[2][1]);
  MUL2(gu[0], gu[1], gu[0], gu[1], gt[0], gt[1]); SB();
  LDA(w4f[46], l16, w4b + 45056, 2048);
  MMA(a3[1][1], w3f[28], bq[5]); SB();
  MUL2(gu[2], gu[3], gu[2], gu[3], gt[2], gt[3]); SB();
  MMA(a3[1][2], w3f[29], bq[5]); SB();
  EXP1(gu[0]); SB();
  asm volatile("s_waitcnt vmcnt(10)" ::: "memory");
  wg_barrier();
  SIGNAL(2); SB();
  MMA(a3[1][0], w3f[30], bq[6]); SB();
  EXP1(gu[1]); SB();
  LDA(w4f[47], l16, w4b + 45056, 3072);
  MMA(a3[1][1], w3f[31], bq[6]); SB();
  EXP1(gu[2]); SB();
  MMA(a3[1][2], w3f[32], bq[6]); SB();
  EXP1(gu[3]);
  FMA2(gu[0], gu[1], gk3[2][3], gk3[2][3]); SB();
  MMA(a3[1][0], w3f[33], bq[7]); SB();
  FMA2(gu[2], gu[3], gk3[2][3], gk3[2][3]); SB();
  LDA(w4f[48], l16, w4b + 49152, 0);
  MMA(a3[1][1], w3f[34], bq[7]); SB();
  RCP1(gu[0]); SB();
  MMA(a3[1][2], w3f[35], bq[7]); SB();
  RCP1(gu[1]); SB();
  asm volatile("" :: "v"(bq[3]));
  bq[4] = LP(v4u, xr + 77824);
  bq[5] = LP(v4u, xr + 78848);
  MMA(a3[1][0], w3f[36], bq[0]); SB();
  RCP1(gu[2]); SB();
  LDA(w4f[49], l16, w4b + 49152, 1024);
  MMA(a3[1][1], w3f[37], bq[0]); SB();
  RCP1(gu[3]); SB();
  MMA(a3[1][2], w3f[38], bq[0]); SB();
  MUL2(gu[0], gu[1], gu[0], gu[1], gt[0], gt[1]);
  MUL2(gu[2], gu[3], gu[2], gu[3], gt[2], gt[3]); SB();
  bq[6] = LP(v4u, xr + 79872);
  bq[7] = LP(v4u, xr + 80896);
  MMA(a3[1][0], w3f[39], bq[1]); SB();
  CVT2(o5[1][0], o5[1][1], gu); SB();
  LDA(w4f[50], l16, w4b + 49152, 2048);
  MMA(a3[1][1], w3f[40], bq[1]); SB();
  ST4(o5[0], o5[1], h_p + 98304); SB();
  MMA(a3[1][2], w3f[41], bq[1]); SB();
  FMA2N(gt[0], gt[1], cs3[2][0], cs3[2][1], gk3[2][0], a3[0][2][0], a3[0][2][1]); SB();
  MMA(a3[1][0], w3f[42], bq[2]); SB();
  FMA2N(gt[2], gt[3], cs3[2][2], cs3[2][3], gk3[2][0], a3[0][2][2], a3[0][2][3]); SB();
  LDA(w4f[51], l16, w4b + 49152, 3072);
  MMA(a3[1][1], w3f[43], bq[2]); SB();
  MUL2(gu[0], gu[1], gt[0], gt[1], gt[0], gt[1]);
  MUL2(gu[2], gu[3], gt[2], gt[3], gt[2], gt[3]); SB();
  MMA(a3[1][2], w3f[44], bq[2]); SB();
  FMA2(gu[0], gu[1], gk3[2][2], gk3[2][1]); SB();
  MMA(a3[1][0], w3f[45], bq[3]); SB();
  FMA2(gu[2], gu[3], gk3[2][2], gk3[2][1]); SB();
  LDA(w4f[52], l16, w4b + 53248, 0);
  MMA(a3[1][1], w3f[46], bq[3]); SB();
  MUL2(gu[0], gu[1], gu[0], gu[1], gt[0], gt[1]); SB();
  MMA(a3[1][2], w3f[47], bq[3]); SB();
  MUL2(gu[2], gu[3], gu[2], gu[3], gt[2], gt[3]); SB();
  asm volatile("" :: "v"(bq[7]));
  MMA(a3[1][0], w3f[48], bq[4]); SB();
  EXP1(gu[0]); SB();
  LDA(w4f[53], l16, w4b + 53248, 1024);
  MMA(a3[1][1], w3f[49], bq[4]); SB();
  EXP1(gu[1]);
  EXP1(gu[2]); SB();
  MMA(a3[1][2], w3f[50], bq[4]); SB();
  EXP1(gu[3]); SB();
  MMA(a3[1][0], w3f[51], bq[5]); SB();
  FMA2(gu[0], gu[1], gk3[2][3], gk3[2][3]); SB();
  LDA(w4f[54], l16, w4b + 53248, 2048);
  MMA(a3[1][1], w3f[52], bq[5]); SB();
  FMA2(gu[2], gu[3], gk3[2][3], gk3[2][3]); SB();
  MMA(a3[1][2], w3f[53], bq[5]); SB();
  RCP1(gu[0]); SB();
  MMA(a3[1][0], w3f[54], bq[6]); SB();
  RCP1(gu[1]);
  RCP1(gu[2]); SB();
  LDA(w4f[55], l16, w4b + 53248, 3072);
  MMA(a3[1][1], w3f[55], bq[6]); SB();
  RCP1(gu[3]); SB();
  MMA(a3[1][2], w3f[56], bq[6]); SB();
  MUL2(gu[0], gu[1], gu[0], gu[1], gt[0], gt[1]); SB();
  MMA(a3[1][0], w3f[57], bq[7]); SB();
  MUL2(gu[2], gu[3], gu[2], gu[3], gt[2], gt[3]); SB();
  LDA(w4f[56], l16, w4b + 57344, 0);
  MMA(a3[1][1], w3f[58], bq[7]); SB();
  CVT2(o5[2][0], o5[2][1], gu); SB();
  MMA(a3[1][2], w3f[59], bq[7]); SB();
  ST2(o5[2], h_s + 98304); SB();
  { const GK gl = {f32x4{gk3[3][0], gk3[3][0], gk3[3][0], gk3[3][0]}, f32x4{gk3[3][1], gk3[3][1], gk3[3][1], gk3[3][1]}, f32x4{gk3[3][2], gk3[3][2], gk3[3][2], gk3[3][2]}, f32x4{gk3[3][3], gk3[3][3], gk3[3][3], gk3[3][3]}};
  POLL(2);
  asm volatile("s_nop 7" : "+v"(a3[1][0]), "+v"(a3[1][1]), "+v"(a3[1][2]));
  LDA(w4f[57], l16, w4b + 57344, 1024);
  LDA(w4f[58], l16, w4b + 57344, 2048);
  LDA(w4f[59], l16, w4b + 57344, 3072); SB();
  ln_gelu_n<3>(&a3[1][0], &cs3[0], gl, &o5[0]);
  ST4(o5[0], o5[1], h_p + 147456);
  ST2(o5[2], h_s + 147456); SB();
  PAD(6); SB();
  }
  asm volatile("s_waitcnt vmcnt(24)" ::: "memory");
  w4f[27] = LP(v4u, redw + 0);
  w4f[28] = LP(v4u, redw + 1024);
  w4f[29] = LP(v4u, redw + 2048);
  w4f[30] = LP(v4u, redw + 3072);
  w4f[31] = LP(v4u, redw + 4096);
  w4f[32] = LP(v4u, redw + 5120);
  w4f[33] = LP(v4u, redw + 6144);
  w4f[34] = LP(v4u, redw + 7168);
  w4f[35] = LP(v4u, redw + 8192);
  w4f[36] = LP(v4u, redw + 9216);
  w4f[37] = LP(v4u, redw + 10240);
  w4f[38] = LP(v4u, redw + 11264);
  w4f[39] = LP(v4u, redw + 12288);
  w4f[40] = LP(v4u, redw + 13312);
  w4f[41] = LP(v4u, redw + 14336);
  ADDR_E
  SB();
  asm volatile("s_waitcnt vmcnt(5)" : "+v"(fv) :: "memory");
  CHECK(2);
  LDVS(af[0], l16, ab4[0] + 0, 0);
  LDVS(af[1], l16, ab4[0] + 0, 1024);
  LDVS(af[2], l16, ab4[0] + 0, 2048);
  LDVS(af[3], l16, ab4[0] + 0, 3072);
  LDVS(af[4], l16, ab4[0] + 4096, 0);
  LDVS(af[5], l16, ab4[0] + 4096, 1024);
  LDVS(af[6], l16, ab4[0] + 4096, 2048); SB();
  asm volatile("s_waitcnt vmcnt(7)" ::: "memory");
  wg_barrier();
  SIGNAL(3); SB();
  LDVS(af[7], l16, ab4[0] + 4096, 3072);
  LDVS(af[8], l16, ab4[0] + 8192, 0);
  LDVS(af[9], l16, ab4[0] + 8192, 1024);
  LDVS(af[10], l16, ab4[0] + 8192, 2048);
  LDVS(af[11], l16, ab4[0] + 8192, 3072);
  LDVS(af[12], l16, ab4[1] + 0, 0);
  LDVS(af[13], l16, ab4[1] + 0, 1024); SB();
  LDVS(af[14], l16, ab4[1] + 0, 2048);
  asm volatile("s_waitcnt vmcnt(15)" : "+v"(af[0]) :: "memory");
  acc[0][0] = mfma(w4f[0], af[0], Z4); SB();
  acc[0][1] = mfma(w4f[1], af[0], Z4); SB();
  acc[0][2] = mfma(w4f[2], af[0], Z4); SB();
  acc[0][3] = mfma(w4f[3], af[0], Z4); SB();
  acc[0][4] = mfma(w4f[4], af[0], Z4); SB();
  LDVS(af[15], l16, ab4[1] + 0, 3072);
  asm volatile("s_waitcnt vmcnt(15)" : "+v"(af[1]) :: "memory");
  acc[0][0] = mfma(w4f[5], af[1], acc[0][0]); SB();
  acc[0][1] = mfma(w4f[6], af[1], acc[0][1]); SB();
  acc[0][2] = mfma(w4f[7], af[1], acc[0][2]); SB();
  acc[0][3] = mfma(w4f[8], af[1], acc[0][3]); SB();
  acc[0][4] = mfma(w4f[9], af[1], acc[0][4]); SB();
  LDVS(af[0], l16, ab4[1] + 4096, 0);
  asm volatile("s_waitcnt vmcnt(15)" : "+v"(af[2]) :: "memory");
  acc[0][0] = mfma(w4f[10], af[2], acc[0][0]); SB();
  acc[0][1] = mfma(w4f[11], af[2], acc[0][1]); SB();
  acc[0][2] = mfma(w4f[12], af[2], acc[0][2]); SB();
  acc[0][3] = mfma(w4f[13], af[2], acc[0][3]); SB();
  acc[0][4] = mfma(w4f[14], af[2], acc[0][4]); SB();
  LDVS(af[1], l16, ab4[1] + 4096, 1024);
  asm volatile("s_waitcnt vmcnt(15)" : "+v"(af[3]) :: "memory");
  acc[0][0] = mfma(w4f[15], af[3], acc[0][0]); SB();
  acc[0][1] = mfma(w4f[16], af[3], acc[0][1]); SB();
  acc[0][2] = mfma(w4f[17], af[3], acc[0][2]); SB();
  acc[0][3] = mfma(w4f[18], af[3], acc[0][3]); SB();
  acc[0][4] = mfma(w4f[19], af[3], acc[0][4]); SB();
  LDVS(af[2], l16, ab4[1] + 4096, 2048);
  asm volatile("s_waitcnt vmcnt(15)" : "+v"(af[4]) :: "memory");
  acc[0][0] = mfma(w4f[20], af[4], acc[0][0]); SB();
  acc[0][1] = mfma(w4f[21], af[4], acc[0][1]); SB();
  acc[0][2] = mfma(w4f[22], af[4], acc[0][2]); SB();
  acc[0][3] = mfma(w4f[23], af[4], acc[0][3]); SB();
  acc[0][4] = mfma(w4f[24], af[4], acc[0][4]); SB();
  LDVS(af[3], l16, ab4[1] + 4096, 3072);
  asm volatile("s_waitcnt vmcnt(15)" : "+v"(af[5]) :: "memory");
  acc[0][0] = mfma(w4f[25], af[5], acc[0][0]); SB();
  acc[0][1] = mfma(w4f[26], af[5], acc[0][1]); SB();
  acc[0][2] = mfma(w4f[27], af[5], acc[0][2]); SB();
  acc[0][3] = mfma(w4f[28], af[5], acc[0][3]); SB();
  acc[0][4] = mfma(w4f[29], af[5], acc[0][4]); SB();
  LDVS(af[4], l16, ab4[1] + 8192, 0);
  asm volatile("s_waitcnt vmcnt(15)" : "+v"(af[6]) :: "memory");
  acc[0][0] = mfma(w4f[30], af[6], acc[0][0]); SB();
  acc[0][1] = mfma(w4f[31], af[6], acc[0][1]); SB();
  acc[0][2] = mfma(w4f[32], af[6], acc[0][2]); SB();
  acc[0][3] = mfma(w4f[33], af[6], acc[0][3]); SB();
  acc[0][4] = mfma(w4f[34], af[6], acc[0][4]); SB();
  LDVS(af[5], l16, ab4[1] + 8192, 1024);
  asm volatile("s_waitcnt vmcnt(14)" : "+v"(af[7]) :: "memory");
  acc[0][0] = mfma(w4f[35], af[7], acc[0][0]); SB();
  acc[0][1] = mfma(w4f[36], af[7], acc[0][1]); SB();
  acc[0][2] = mfma(w4f[37], af[7], acc[0][2]); SB();
  acc[0][3] = mfma(w4f[38], af[7], acc[0][3]); SB();
  acc[0][4] = mfma(w4f[39], af[7], acc[0][4]); SB();
  LDVS(af[6], l16, ab4[1] + 8192, 2048);
  asm volatile("s_waitcnt vmcnt(14)" : "+v"(af[8]) :: "memory");
  acc[0][0] = mfma(w4f[40], af[8], acc[0][0]); SB();
  acc[0][1] = mfma(w4f[41], af[8], acc[0][1]); SB();
  acc[0][2] = mfma(w4f[42], af[8], acc[0][2]); SB();
  acc[0][3] = mfma(w4f[43], af[8], acc[0][3]); SB();
  acc[0][4] = mfma(w4f[44], af[8], acc[0][4]); SB();
  LDVS(af[7], l16, ab4[1] + 8192, 3072);
  asm volatile("s_waitcnt vmcnt(14)" : "+v"(af[9]) :: "memory");
  acc[0][0] = mfma(w4f[45], af[9], acc[0][0]); SB();
  acc[0][1] = mfma(w4f[46], af[9], acc[0][1]); SB();
  acc[0][2] = mfma(w4f[47], af[9], acc[0][2]); SB();
  acc[0][3] = mfma(w4f[48], af[9], acc[0][3]); SB();
  acc[0][4] = mfma(w4f[49], af[9], acc[0][4]); SB();
  asm volatile("s_waitcnt vmcnt(13)" : "+v"(af[10]) :: "memory");
  acc[0][0] = mfma(w4f[50], af[10], acc[0][0]); SB();
  acc[0][1] = mfma(w4f[51], af[10], acc[0][1]); SB();
  acc[0][2] = mfma(w4f[52], af[10], acc[0][2]); SB();
  acc[0][3] = mfma(w4f[53], af[10], acc[0][3]); SB();
  acc[0][4] = mfma(w4f[54], af[10], acc[0][4]); SB();
  asm volatile("s_waitcnt vmcnt(12)" : "+v"(af[11]) :: "memory");
  acc[0][0] = mfma(w4f[55], af[11], acc[0][0]); SB();
  acc[0][1] = mfma(w4f[56], af[11], acc[0][1]); SB();
  acc[0][2] = mfma(w4f[57], af[11], acc[0][2]); SB();
  acc[0][3] = mfma(w4f[58], af[11], acc[0][3]); SB();
  acc[0][4] = mfma(w4f[59], af[11], acc[0][4]); SB();
  POLL(3);
  asm volatile("s_waitcnt vmcnt(12)" : "+v"(af[12]) :: "memory");
  acc[1][0] = mfma(w4f[0], af[12], Z4); SB();
  acc[1][1] = mfma(w4f[1], af[12], Z4); SB();
  acc[1][2] = mfma(w4f[2], af[12], Z4); SB();
  LP(f32x4, redw4 + 0) = acc[0][0]; SB();
  acc[1][3] = mfma(w4f[3], af[12], Z4); SB();
  acc[1][4] = mfma(w4f[4], af[12], Z4); SB();
  asm volatile("s_waitcnt vmcnt(11)" : "+v"(af[13]) :: "memory");
  acc[1][0] = mfma(w4f[5], af[13], acc[1][0]); SB();
  acc[1][1] = mfma(w4f[6], af[13], acc[1][1]); SB();
  acc[1][2] = mfma(w4f[7], af[13], acc[1][2]); SB();
  acc[1][3] = mfma(w4f[8], af[13], acc[1][3]); SB();
  acc[1][4] = mfma(w4f[9], af[13], acc[1][4]); SB();
  asm volatile("s_waitcnt vmcnt(10)" : "+v"(af[14]) :: "memory");
  acc[1][0] = mfma(w4f[10], af[14], acc[1][0]); SB();
  acc[1][1] = mfma(w4f[11], af[14], acc[1][1]); SB();
  acc[1][2] = mfma(w4f[12], af[14], acc[1][2]); SB();
  LP(f32x4, redw4 + 1024) = acc[0][1]; SB();
  acc[1][3] = mfma(w4f[13], af[14], acc[1][3]); SB();
  acc[1][4] = mfma(w4f[14], af[14], acc[1][4]); SB();
  asm volatile("s_waitcnt vmcnt(9)" : "+v"(af[15]) :: "memory");
  acc[1][0] = mfma(w4f[15], af[15], acc[1][0]); SB();
  acc[1][1] = mfma(w4f[16], af[15], acc[1][1]); SB();
  acc[1][2] = mfma(w4f[17], af[15], acc[1][2]); SB();
  acc[1][3] = mfma(w4f[18], af[15], acc[1][3]); SB();
  acc[1][4] = mfma(w4f[19], af[15], acc[1][4]); SB();
  asm volatile("s_waitcnt vmcnt(8)" : "+v"(af[0]) :: "memory");
  acc[1][0] = mfma(w4f[20], af[0], acc[1][0]); SB();
  acc[1][1] = mfma(w4f[21], af[0], acc[1][1]); SB();
  acc[1][2] = mfma(w4f[22], af[0], acc[1][2]); SB();
  LP(f32x4, redw4 + 2048) = acc[0][2]; SB();
  acc[1][3] = mfma(w4f[23], af[0], acc[1][3]); SB();
  acc[1][4] = mfma(w4f[24], af[0], acc[1][4]); SB();
  asm volatile("s_waitcnt vmcnt(7)" : "+v"(af[1]) :: "memory");
  acc[1][0] = mfma(w4f[25], af[1], acc[1][0]); SB();
  acc[1][1] = mfma(w4f[26], af[1], acc[1][1]); SB();
  acc[1][2] = mfma(w4f[27], af[1], acc[1][2]); SB();
  acc[1][3] = mfma(w4f[28], af[1], acc[1][3]); SB();
  acc[1][4] = mfma(w4f[29], af[1], acc[1][4]); SB();
  asm volatile("s_waitcnt vmcnt(0)" : "+v"(fv) :: "memory");
  CHECK(3);
  LDVS(af[8], l16, ab4[2] + 0, 0);
  LDVS(af[9], l16, ab4[2] + 0, 1024);
  asm volatile("" : "+v"(af[2]));
  acc[1][0] = mfma(w4f[30], af[2], acc[1][0]); SB();
  acc[1][1] = mfma(w4f[31], af[2], acc[1][1]); SB();
  acc[1][2] = mfma(w4f[32], af[2], acc[1][2]); SB();
  LP(f32x4, redw4 + 3072) = acc[0][3]; SB();
  acc[1][3] = mfma(w4f[33], af[2], acc[1][3]); SB();
  acc[1][4] = mfma(w4f[34], af[2], acc[1][4]); SB();
  LDVS(af[10], l16, ab4[2] + 0, 2048);
  LDVS(af[11], l16, ab4[2] + 0, 3072);
  asm volatile("" : "+v"(af[3]));
  acc[1][0] = mfma(w4f[35], af[3], acc[1][0]); SB();
  acc[1][1] = mfma(w4f[36], af[3], acc[1][1]); SB();
  acc[1][2] = mfma(w4f[37], af[3], acc[1][2]); SB();
  acc[1][3] = mfma(w4f[38], af[3], acc[1][3]); SB();
  acc[1][4] = mfma(w4f[39], af[3], acc[1][4]); SB();
  LDVS(af[12], l16, ab4[2] + 4096, 0);
  LDVS(af[13], l16, ab4[2] + 4096, 1024);
  asm volatile("" : "+v"(af[4]));
  acc[1][0] = mfma(w4f[40], af[4], acc[1][0]); SB();
  acc[1][1] = mfma(w4f[41], af[4], acc[1][1]); SB();
  acc[1][2] = mfma(w4f[42], af[4], acc[1][2]); SB();
  LP(f32x4, redw4 + 4096) = acc[0][4]; SB();
  acc[1][3] = mfma(w4f[43], af[4], acc[1][3]); SB();
  acc[1][4] = mfma(w4f[44], af[4], acc[1][4]); SB();
  LDVS(af[14], l16, ab4[2] + 4096, 2048);
  LDVS(af[15], l16, ab4[2] + 4096, 3072);
  asm volatile("" : "+v"(af[5]));
  acc[1][0] = mfma(w4f[45], af[5], acc[1][0]); SB();
  acc[1][1] = mfma(w4f[46], af[5], acc[1][1]); SB();
  acc[1][2] = mfma(w4f[47], af[5], acc[1][2]); SB();
  acc[1][3] = mfma(w4f[48], af[5], acc[1][3]); SB();
  acc[1][4] = mfma(w4f[49], af[5], acc[1][4]); SB();
  LDVS(af[0], l16, ab4[2] + 8192, 0);
  LDVS(af[1], l16, ab4[2] + 8192, 1024);
  asm volatile("" : "+v"(af[6]));
  acc[1][0] = mfma(w4f[50], af[6], acc[1][0]); SB();
  acc[1][1] = mfma(w4f[51], af[6], acc[1][1]); SB();
  acc[1][2] = mfma(w4f[52], af[6], acc[1][2]); SB();
  acc[1][3] = mfma(w4f[53], af[6], acc[1][3]); SB();
  acc[1][4] = mfma(w4f[54], af[6], acc[1][4]); SB();
  LDVS(af[2], l16, ab4[2] + 8192, 2048);
  LDVS(af[3], l16, ab4[2] + 8192, 3072);
  asm volatile("" : "+v"(af[7]));
  acc[1][0] = mfma(w4f[55], af[7], acc[1][0]); SB();
  acc[1][1] = mfma(w4f[56], af[7], acc[1][1]); SB();
  acc[1][2] = mfma(w4f[57], af[7], acc[1][2]); SB();
  acc[1][3] = mfma(w4f[58], af[7], acc[1][3]); SB();
  acc[1][4] = mfma(w4f[59], af[7], acc[1][4]); SB();
  LDVS(af[4], l16, ab4[3] + 0, 0);
  LDVS(af[5], l16, ab4[3] + 0, 1024);
  asm volatile("s_waitcnt vmcnt(13)" : "+v"(af[8]) :: "memory");
  acc[0][0] = mfma(w4f[0], af[8], Z4); SB();
  acc[0][1] = mfma(w4f[1], af[8], Z4); SB();
  acc[0][2] = mfma(w4f[2], af[8], Z4); SB();
  LP(f32x4, redw4 + 5120) = acc[1][0]; SB();
  acc[0][3] = mfma(w4f[3], af[8], Z4); SB();
  acc[0][4] = mfma(w4f[4], af[8], Z4); SB();
  LDVS(af[6], l16, ab4[3] + 0, 2048);
  LDVS(af[7], l16, ab4[3] + 0, 3072);
  asm volatile("s_waitcnt vmcnt(14)" : "+v"(af[9]) :: "memory");
  acc[0][0] = mfma(w4f[5], af[9], acc[0][0]); SB();
  acc[0][1] = mfma(w4f[6], af[9], acc[0][1]); SB();
  acc[0][2] = mfma(w4f[7], af[9], acc[0][2]); SB();
  acc[0][3] = mfma(w4f[8], af[9], acc[0][3]); SB();
  acc[0][4] = mfma(w4f[9], af[9], acc[0][4]); SB();
  LDVS(af[8], l16, ab4[3] + 4096, 0);
  asm volatile("s_waitcnt vmcnt(14)" : "+v"(af[10]) :: "memory");
  acc[0][0] = mfma(w4f[10], af[10], acc[0][0]); SB();
  acc[0][1] = mfma(w4f[11], af[10], acc[0][1]); SB();
  acc[0][2] = mfma(w4f[12], af[10], acc[0][2]); SB();
  LP(f32x4, redw4 + 6144) = acc[1][1]; SB();
  acc[0][3] = mfma(w4f[13], af[10], acc[0][3]); SB();
  acc[0][4] = mfma(w4f[14], af[10], acc[0][4]); SB();
  LDVS(af[9], l16, ab4[3] + 4096, 1024);
  asm volatile("s_waitcnt vmcnt(14)" : "+v"(af[11]) :: "memory");
  acc[0][0] = mfma(w4f[15], af[11], acc[0][0]); SB();
  acc[0][1] = mfma(w4f[16], af[11], acc[0][1]); SB();
  acc[0][2] = mfma(w4f[17], af[11], acc[0][2]); SB();
  acc[0][3] = mfma(w4f[18], af[11], acc[0][3]); SB();
  acc[0][4] = mfma(w4f[19], af[11], acc[0][4]); SB();
  LDVS(af[10], l16, ab4[3] + 4096, 2048);
  asm volatile("s_waitcnt vmcnt(14)" : "+v"(af[12]) :: "memory");
  acc[0][0] = mfma(w4f[20], af[12], acc[0][0]); SB();
  acc[0][1] = mfma(w4f[21], af[12], acc[0][1]); SB();
  acc[0][2] = mfma(w4f[22], af[12], acc[0][2]); SB();
  LP(f32x4, redw4 + 7168) = acc[1][2]; SB();
  acc[0][3] = mfma(w4f[23], af[12], acc[0][3]); SB();
  acc[0][4] = mfma(w4f[24], af[12], acc[0][4]); SB();
  LDVS(af[11], l16, ab4[3] + 4096, 3072);
  asm volatile("s_waitcnt vmcnt(14)" : "+v"(af[13]) :: "memory");
  acc[0][0] = mfma(w4f[25], af[13], acc[0][0]); SB();
  acc[0][1] = mfma(w4f[26], af[13], acc[0][1]); SB();
  acc[0][2] = mfma(w4f[27], af[13], acc[0][2]); SB();
  acc[0][3] = mfma(w4f[28], af[13], acc[0][3]); SB();
  acc[0][4] = mfma(w4f[29], af[13], acc[0][4]); SB();
  LDVS(af[12], l16, ab4[3] + 8192, 0);
  asm volatile("s_waitcnt vmcnt(14)" : "+v"(af[14]) :: "memory");
  acc[0][0] = mfma(w4f[30], af[14], acc[0][0]); SB();
  acc[0][1] = mfma(w4f[31], af[14], acc[0][1]); SB();
  acc[0][2] = mfma(w4f[32], af[14], acc[0][2]); SB();
  LP(f32x4, redw4 + 8192) = acc[1][3]; SB();
  acc[0][3] = mfma(w4f[33], af[14], acc[0][3]); SB();
  acc[0][4] = mfma(w4f[34], af[14], acc[0][4]); SB();
  LDVS(af[13], l16, ab4[3] + 8192, 1024);
  asm volatile("s_waitcnt vmcnt(14)" : "+v"(af[15]) :: "memory");
  acc[0][0] = mfma(w4f[35], af[15], acc[0][0]); SB();
  acc[0][1] = mfma(w4f[36], af[15], acc[0][1]); SB();
  acc[0][2] = mfma(w4f[37], af[15], acc[0][2]); SB();
  acc[0][3] = mfma(w4f[38], af[15], acc[0][3]); SB();
  acc[0][4] = mfma(w4f[39], af[15], acc[0][4]); SB();
  LDVS(af[14], l16, ab4[3] + 8192, 2048);
  asm volatile("s_waitcnt vmcnt(14)" : "+v"(af[0]) :: "memory");
  acc[0][0] = mfma(w4f[40], af[0], acc[0][0]); SB();
  acc[0][1] = mfma(w4f[41], af[0], acc[0][1]); SB();
  acc[0][2] = mfma(w4f[42], af[0], acc[0][2]); SB();
  LP(f32x4, redw4 + 9216) = acc[1][4]; SB();
  acc[0][3] = mfma(w4f[43], af[0], acc[0][3]); SB();
  acc[0][4] = mfma(w4f[44], af[0], acc[0][4]); SB();
  LDVS(af[15], l16, ab4[3] + 8192, 3072);
  asm volatile("s_waitcnt vmcnt(14)" : "+v"(af[1]) :: "memory");
  acc[0][0] = mfma(w4f[45], af[1], acc[0][0]); SB();
  acc[0][1] = mfma(w4f[46], af[1], acc[0][1]); SB();
  acc[0][2] = mfma(w4f[47], af[1], acc[0][2]); SB();
  acc[0][3] = mfma(w4f[48], af[1], acc[0][3]); SB();
  acc[0][4] = mfma(w4f[49], af[1], acc[0][4]); SB();
  asm volatile("s_waitcnt vmcnt(13)" : "+v"(af[2]) :: "memory");
  acc[0][0] = mfma(w4f[50], af[2], acc[0][0]); SB();
  acc[0][1] = mfma(w4f[51], af[2], acc[0][1]); SB();
  acc[0][2] = mfma(w4f[52], af[2], acc[0][2]); SB();
  acc[0][3] = mfma(w4f[53], af[2], acc[0][3]); SB();
  acc[0][4] = mfma(w4f[54], af[2], acc[0][4]); SB();
  asm volatile("s_waitcnt vmcnt(12)" : "+v"(af[3]) :: "memory");
  acc[0][0] = mfma(w4f[55], af[3], acc[0][0]); SB();
  acc[0][1] = mfma(w4f[56], af[3], acc[0][1]); SB();
  acc[0][2] = mfma(w4f[57], af[3], acc[0][2]); SB();
  acc[0][3] = mfma(w4f[58], af[3], acc[0][3]); SB();
  acc[0][4] = mfma(w4f[59], af[3], acc[0][4]); SB();
  asm volatile("s_waitcnt vmcnt(11)" : "+v"(af[4]) :: "memory");
  acc[1][0] = mfma(w4f[0], af[4], Z4); SB();
  acc[1][1] = mfma(w4f[1], af[4], Z4); SB();
  acc[1][2] = mfma(w4f[2], af[4], Z4); SB();
  LP(f32x4, redw4 + 10240) = acc[0][0]; SB();
  acc[1][3] = mfma(w4f[3], af[4], Z4); SB();
  acc[1][4] = mfma(w4f[4], af[4], Z4); SB();
  asm volatile("s_waitcnt vmcnt(10)" : "+v"(af[5]) :: "memory");
  acc[1][0] = mfma(w4f[5], af[5], acc[1][0]); SB();
  acc[1][1] = mfma(w4f[6], af[5], acc[1][1]); SB();
  LP(f32x4, redw4 + 11264) = acc[0][1]; SB();
  acc[1][2] = mfma(w4f[7], af[5], acc[1][2]); SB();
  acc[1][3] = mfma(w4f[8], af[5], acc[1][3]); SB();
  acc[1][4] = mfma(w4f[9], af[5], acc[1][4]); SB();
  asm volatile("s_waitcnt vmcnt(9)" : "+v"(af[6]) :: "memory");
  acc[1][0] = mfma(w4f[10], af[6], acc[1][0]); SB();
  LP(f32x4, redw4 + 12288) = acc[0][2]; SB();
  acc[1][1] = mfma(w4f[11], af[6], acc[1][1]); SB();
  acc[1][2] = mfma(w4f[12], af[6], acc[1][2]); SB();
  acc[1][3] = mfma(w4f[13], af[6], acc[1][3]); SB();
  acc[1][4] = mfma(w4f[14], af[6], acc[1][4]); SB();
  LP(f32x4, redw4 + 13312) = acc[0][3]; SB();
  asm volatile("s_waitcnt vmcnt(8)" : "+v"(af[7]) :: "memory");
  acc[1][0] = mfma(w4f[15], af[7], acc[1][0]); SB();
  acc[1][1] = mfma(w4f[16], af[7], acc[1][1]); SB();
  acc[1][2] = mfma(w4f[17], af[7], acc[1][2]); SB();
  acc[1][3] = mfma(w4f[18], af[7], acc[1][3]); SB();
  LP(f32x4, redw4 + 14336) = acc[0][4]; SB();
  acc[1][4] = mfma(w4f[19], af[7], acc[1][4]); SB();
  asm volatile("s_waitcnt vmcnt(7)" : "+v"(af[8]) :: "memory");
  acc[1][0] = mfma(w4f[20], af[8], acc[1][0]); SB();
  acc[1][1] = mfma(w4f[21], af[8], acc[1][1]); SB();
  acc[1][2] = mfma(w4f[22], af[8], acc[1][2]); SB();
  acc[1][3] = mfma(w4f[23], af[8], acc[1][3]); SB();
  acc[1][4] = mfma(w4f[24], af[8], acc[1][4]); SB();
  wg_barrier();
  p15[0] = LP(f32x4, redr[0] + 0);
  p15[1] = LP(f32x4, redr[0] + 1024);
  p15[2] = LP(f32x4, redr[0] + 2048);
  p15[3] = LP(f32x4, redr[0] + 3072);
  p15[4] = LP(f32x4, redr[0] + 4096);
  p15[5] = LP(f32x4, redr[0] + 40960);
  p15[6] = LP(f32x4, redr[0] + 41984);
  p15[7] = LP(f32x4, redr[0] + 43008);
  p15[8] = LP(f32x4, redr[0] + 44032);
  p15[9] = LP(f32x4, redr[0] + 45056);
  asm volatile("s_waitcnt vmcnt(6)" : "+v"(af[9]) :: "memory");
  acc[1][0] = mfma(w4f[25], af[9], acc[1][0]); SB();
  acc[1][1] = mfma(w4f[26], af[9], acc[1][1]); SB();
  acc[1][2] = mfma(w4f[27], af[9], acc[1][2]); SB();
  acc[1][3] = mfma(w4f[28], af[9], acc[1][3]); SB();
  acc[1][4] = mfma(w4f[29], af[9], acc[1][4]); SB();
  asm volatile("s_waitcnt vmcnt(5)" : "+v"(af[10]) :: "memory");
  acc[1][0] = mfma(w4f[30], af[10], acc[1][0]); SB();
  ADD2(base[0][0], base[0][1], p15[0][0], p15[0][1]); SB();
  acc[1][1] = mfma(w4f[31], af[10], acc[1][1]); SB();
  ADD2(base[0][2], base[0][3], p15[0][2], p15[0][3]); SB();
  acc[1][2] = mfma(w4f[32], af[10], acc[1][2]); SB();
  ADD2(base[1][0], base[1][1], p15[1][0], p15[1][1]); SB();
  acc[1][3] = mfma(w4f[33], af[10], acc[1][3]); SB();
  ADD2(base[1][2], base[1][3], p15[1][2], p15[1][3]); SB();
  acc[1][4] = mfma(w4f[34], af[10], acc[1][4]); SB();
  ADD2(base[2][0], base[2][1], p15[2][0], p15[2][1]); SB();
  asm volatile("s_waitcnt vmcnt(4)" : "+v"(af[11]) :: "memory");
  acc[1][0] = mfma(w4f[35], af[11], acc[1][0]); SB();
  ADD2(base[2][2], base[2][3], p15[2][2], p15[2][3]); SB();
  acc[1][1] = mfma(w4f[36], af[11], acc[1][1]); SB();
  ADD2(base[3][0], base[3][1], p15[3][0], p15[3][1]); SB();
  acc[1][2] = mfma(w4f[37], af[11], acc[1][2]); SB();
  ADD2(base[3][2], base[3][3], p15[3][2], p15[3][3]); SB();
  acc[1][3] = mfma(w4f[38], af[11], acc[1][3]); SB();
  ADD2(base[4][0], base[4][1], p15[4][0], p15[4][1]); SB();
  acc[1][4] = mfma(w4f[39], af[11], acc[1][4]); SB();
  ADD2(base[4][2], base[4][3], p15[4][2], p15[4][3]); SB();
  asm volatile("s_waitcnt vmcnt(3)" : "+v"(af[12]) :: "memory");
  acc[1][0] = mfma(w4f[40], af[12], acc[1][0]); SB();
  ADD2(base[0][0], base[0][1], p15[5][0], p15[5][1]); SB();
  acc[1][1] = mfma(w4f[41], af[12], acc[1][1]); SB();
  ADD2(base[0][2], base[0][3], p15[5][2], p15[5][3]); SB();
  acc[1][2] = mfma(w4f[42], af[12], acc[1][2]); SB();
  ADD2(base[1][0], base[1][1], p15[6][0], p15[6][1]); SB();
  acc[1][3] = mfma(w4f[43], af[12], acc[1][3]); SB();
  ADD2(base[1][2], base[1][3], p15[6][2], p15[6][3]); SB();
  acc[1][4] = mfma(w4f[44], af[12], acc[1][4]); SB();
  ADD2(base[2][0], base[2][1], p15[7][0], p15[7][1]); SB();
  asm volatile("s_waitcnt vmcnt(2)" : "+v"(af[13]) :: "memory");
  acc[1][0] = mfma(w4f[45], af[13], acc[1][0]); SB();
  ADD2(base[2][2], base[2][3], p15[7][2], p15[7][3]); SB();
  acc[1][1] = mfma(w4f[46], af[13], acc[1][1]); SB();
  ADD2(base[3][0], base[3][1], p15[8][0], p15[8][1]); SB();
  acc[1][2] = mfma(w4f[47], af[13], acc[1][2]); SB();
  ADD2(base[3][2], base[3][3], p15[8][2], p15[8][3]); SB();
  acc[1][3] = mfma(w4f[48], af[13], acc[1][3]); SB();
  ADD2(base[4][0], base[4][1], p15[9][0], p15[9][1]); SB();
  acc[1][4] = mfma(w4f[49], af[13], acc[1][4]); SB();
  ADD2(base[4][2], base[4][3], p15[9][2], p15[9][3]); SB();
  asm volatile("s_waitcnt vmcnt(1)" : "+v"(af[14]) :: "memory");
  acc[1][0] = mfma(w4f[50], af[14], acc[1][0]); SB();
  acc[1][1] = mfma(w4f[51], af[14], acc[1][1]); SB();
  acc[1][2] = mfma(w4f[52], af[14], acc[1][2]); SB();
  acc[1][3] = mfma(w4f[53], af[14], acc[1][3]); SB();
  acc[1][4] = mfma(w4f[54], af[14], acc[1][4]); SB();
  asm volatile("s_waitcnt vmcnt(0)" : "+v"(af[15]) :: "memory");
  acc[1][0] = mfma(w4f[55], af[15], acc[1][0]); SB();
  acc[1][1] = mfma(w4f[56], af[15], acc[1][1]); SB();
  acc[1][2] = mfma(w4f[57], af[15], acc[1][2]); SB();
  acc[1][3] = mfma(w4f[58], af[15], acc[1][3]); SB();
  acc[1][4] = mfma(w4f[59], af[15], acc[1][4]); SB();
  asm volatile("s_branch 39\n\t.fill 39, 4, 0xbf800000"); SB();
  LP(f32x4, redw4 + 15360) = acc[1][0];
  LP(f32x4, redw4 + 16384) = acc[1][1];
  LP(f32x4, redw4 + 17408) = acc[1][2];
  LP(f32x4, redw4 + 18432) = acc[1][3];
  LP(f32x4, redw4 + 19456) = acc[1][4];
  wg_barrier();
  p15[0] = LP(f32x4, redr[1] + 0); p15[5] = LP(f32x4, redr[1] + 40960);
  p15[1] = LP(f32x4, redr[1] + 1024); p15[6] = LP(f32x4, redr[1] + 41984);
  p15[2] = LP(f32x4, redr[1] + 2048); p15[7] = LP(f32x4, redr[1] + 43008);
  p15[3] = LP(f32x4, redr[1] + 3072); p15[8] = LP(f32x4, redr[1] + 44032);
  p15[4] = LP(f32x4, redr[1] + 4096); p15[9] = LP(f32x4, redr[1] + 45056); SB();
  PAD(7); SB();
  base[0] += p15[0] + p15[5];
  o5[0] = v2u{pk2(base[0][0], base[0][1]), pk2(base[0][2], base[0][3])};
  base[1] += p15[1] + p15[6];
  o5[1] = v2u{pk2(base[1][0], base[1][1]), pk2(base[1][2], base[1][3])};
  base[2] += p15[2] + p15[7];
  o5[2] = v2u{pk2(base[2][0], base[2][1]), pk2(base[2][2], base[2][3])};
  base[3] += p15[3] + p15[8];
  o5[3] = v2u{pk2(base[3][0], base[3][1]), pk2(base[3][2], base[3][3])};
  base[4] += p15[4] + p15[9];
  o5[4] = v2u{pk2(base[4][0], base[4][1]), pk2(base[4][2], base[4][3])};
  __builtin_amdgcn_raw_buffer_store_b128(v4u{o5[0][0], o5[0][1], o5[1][0], o5[1][1]}, ro, f_p, 0, 0);
  __builtin_amdgcn_raw_buffer_store_b128(v4u{o5[2][0], o5[2][1], o5[3][0], o5[3][1]}, ro, f_p + 64, 0, 0);
  __builtin_amdgcn_raw_buffer_store_b64(o5[4], ro, f_s, 0, 0);
}

__global__ void k_end(Ptrs P) {}

extern "C" void kernel_launch(void* const* d_in, const int* in_sizes, int n_in, void* d_out, int out_size,
                              void* d_ws, size_t ws_size, hipStream_t stream) {
  Ptrs P;
  P.x = (const u16*)d_in[0]; P.are = (const u16*)d_in[1]; P.aim = (const u16*)d_in[2];
  P.bre = (const u16*)d_in[3]; P.bim = (const u16*)d_in[4]; P.cre = (const u16*)d_in[5];
  P.cim = (const u16*)d_in[6]; P.logdt = (const u16*)d_in[7]; P.dskip = (const u16*)d_in[8];
  P.w1 = (const u16*)d_in[10]; P.w2 = (const u16*)d_in[11]; P.w3 = (const u16*)d_in[12]; P.w4 = (const u16*)d_in[13];
  P.out = (u16*)d_out; P.ws = (char*)d_ws;
  (void)hipFuncSetAttribute((const void*)k_mlp, hipFuncAttributeMaxDynamicSharedMemorySize, ML_LDS);
  k_prep<<<PREP_LN + PREP_KG + PREP_PK, 256, PREP_LDS, stream>>>(P);
  k_mlp<<<256, 256, ML_LDS, stream>>>(P);
}
```
